```python
import torch

def _softmax(x, axis=-1):
    return torch.softmax(x, dim=axis)
def _gelu(x):
    return torch.nn.functional.gelu(x, approximate="tanh")
def _silu(x):
    return torch.nn.functional.silu(x)
def _sigmoid(x):
    return torch.sigmoid(x)
def _layernorm(x, axis=-1, eps=1e-5):
    m = x.mean(dim=axis, keepdim=True)
    v = x.var(dim=axis, keepdim=True, unbiased=False)
    return (x - m) / torch.sqrt(v + eps)
def _scatter_add(tgt, idx, src, axis=0):
    return tgt.clone().index_add_(axis, idx, src)
def _segment_sum(src, seg, n):
    out = torch.zeros((n,) + src.shape[1:], dtype=src.dtype)
    return out.index_add_(0, seg, src)
def _pad(x, pw):
    flat = tuple(v for pair in reversed(pw) for v in pair)
    return torch.nn.functional.pad(x, flat)

kernel_name = 'gated_mix__sliding_local_dilated__mixer_token_mlp__prompt'

def setup_inputs():

    _w = (lambda a: (lambda t: t.to(torch.bfloat16) if t.is_floating_point() else t)(torch.as_tensor(a)))
    def _A_setup():
        B, S, D = 1, 2048, 512
        return {
            "x": _w((lambda *s: torch.randn(*s, dtype=torch.bfloat16) * 0.05)(B, S, D)),
            "wqkv": _w((lambda *s: torch.randn(*s, dtype=torch.bfloat16) * 0.8)(D, 3 * D)),
            "wo": _w((lambda *s: torch.randn(*s, dtype=torch.bfloat16) * 0.05)(D, D)),
        }
    _A_in = _A_setup()

    def _B_setup():
        B, S, D, F = 1, 2048, 512, 1792
        return {
            "x": _w((lambda *s: torch.randn(*s, dtype=torch.bfloat16) * 0.05)(B, S, D)),
            "w1": _w((lambda *s: torch.randn(*s, dtype=torch.bfloat16) * 1.6)(S, F) * (S ** -0.5)),
            "b1": _w((lambda *s: torch.randn(*s, dtype=torch.bfloat16) * 12.8)(F) * 0.02),
            "w2": _w((lambda *s: torch.randn(*s, dtype=torch.bfloat16) * 0.4)(F, S) * (F ** -0.5)),
            "b2": _w((lambda *s: torch.randn(*s, dtype=torch.bfloat16) * 6.4)(S) * 0.02),
        }
    _B_in = _B_setup()

    return {
        "A_x": _A_in["x"],
        "A_wqkv": _A_in["wqkv"],
        "A_wo": _A_in["wo"],
        "B_x": _B_in["x"],
        "B_w1": _B_in["w1"],
        "B_b1": _B_in["b1"],
        "B_w2": _B_in["w2"],
        "B_b2": _B_in["b2"],
        "gate_w": _w((lambda *s: torch.randn(*s, dtype=torch.bfloat16) * 0.05)(512) * 16.0),
    }

def reference(A_x, A_wqkv, A_wo, B_x, B_w1, B_b1, B_w2, B_b2, gate_w):
    def _A_ref(x, wqkv, wo):
        B, S, D = x.shape
        H = 32; Dh = D // H
        W, dil = 8, 2
        qkv = (lambda x, *d: x.permute(*d))((x @ wqkv).reshape(B, S, 3, H, Dh), 2, 0, 3, 1, 4)
        q, k, v = qkv[0], qkv[1], qkv[2]
        scale = Dh ** -0.5
        pos = torch.arange(S)
        off = torch.arange(W) * dil
        idx = pos[:, None] - off[None, :]
        mask = idx >= 0
        idx_c = torch.where(mask, idx, 0).reshape(-1)
        kg = k[:, :, idx_c, :].reshape(B, H, S, W, Dh)
        vg = v[:, :, idx_c, :].reshape(B, H, S, W, Dh)
        logits = torch.einsum("bhsd,bhswd->bhsw", q, kg) * scale
        logits = torch.where(mask[None, None, :, :], logits, -1e9)
        attn = _softmax(logits, axis=-1)
        o = torch.einsum("bhsw,bhswd->bhsd", attn, vg)
        return (lambda x, *d: x.permute(*d))(o, 0, 2, 1, 3).reshape(B, S, D) @ wo

    def _B_ref(x, w1, b1, w2, b2):
        B, S, D = x.shape
        h = _layernorm(x, axis=-1)
        u = torch.einsum("bsd,sf->bdf", h, w1) + b1
        u = _gelu(u)
        y = torch.einsum("bdf,fs->bds", u, w2) + b2
        return (lambda x, *d: x.permute(*d))(y, 0, 2, 1)

    x = A_x
    g = _sigmoid((x * gate_w).sum(-1, keepdims=True))
    a = _A_ref(x, A_wqkv, A_wo)
    b = _B_ref(x, B_w1, B_b1, B_w2, B_b2)
    return g * a + (1 - g) * b
```

```cpp
#include <hip/hip_runtime.h>
#include <hip/hip_bf16.h>
#include <stdint.h>

typedef __attribute__((ext_vector_type(8))) short bf16x8;
typedef __attribute__((ext_vector_type(4))) short s16x4;
typedef __attribute__((ext_vector_type(4))) float f32x4;
typedef __attribute__((ext_vector_type(4))) unsigned u32x4;
typedef __attribute__((ext_vector_type(2))) unsigned u32x2;
typedef __attribute__((ext_vector_type(2))) __bf16 bf2;
typedef unsigned short u16;

#define AS1 __attribute__((address_space(1)))
#define AS3 __attribute__((address_space(3)))

constexpr int S_ = 2048, D_ = 512, F_ = 1792;

__device__ __forceinline__ void glds16(unsigned voff, const void* sbase, unsigned lds) {
  unsigned keep;
  lds = __builtin_amdgcn_readfirstlane(lds);
  asm volatile("s_mov_b32 %0, m0\n\ts_mov_b32 m0, %3\n\ts_nop 0\n\tglobal_load_lds_dwordx4 %1, %2\n\ts_mov_b32 m0, %0"
               : "=&s"(keep) : "v"(voff), "s"(sbase), "s"(lds) : "memory");
}
__device__ __forceinline__ unsigned lds_addr(const char* p) { return (unsigned)(size_t)(const AS3 char*)p; }
#define WAIT_V(n) asm volatile("s_waitcnt vmcnt(%0)" ::"n"(n) : "memory")
#define WAIT_L0() asm volatile("s_waitcnt lgkmcnt(0)" ::: "memory")
#define BAR() asm volatile("s_barrier" ::: "memory")

__device__ __forceinline__ unsigned pack2(float a, float b) {
  bf2 v = {(__bf16)a, (__bf16)b};
  return __builtin_bit_cast(unsigned, v);
}
__device__ __forceinline__ float rbf(float a) { return (float)(__bf16)a; }
__device__ __forceinline__ float lo16(unsigned u) { return __builtin_bit_cast(float, u << 16); }
__device__ __forceinline__ float hi16(unsigned u) { return __builtin_bit_cast(float, u & 0xffff0000u); }

__device__ __forceinline__ bf16x8 ld_b128(const char* p) { return *(const AS3 bf16x8*)(p); }
__device__ __forceinline__ bf16x8 ld_tr2(const char* p, int t_off) {
  s16x4 lo = __builtin_amdgcn_ds_read_tr16_b64_v4i16((AS3 s16x4*)(p));
  s16x4 hi = __builtin_amdgcn_ds_read_tr16_b64_v4i16((AS3 s16x4*)(p + t_off));
  return __builtin_shufflevector(lo, hi, 0, 1, 2, 3, 4, 5, 6, 7);
}
#define MFMA(a, b, c) __builtin_amdgcn_mfma_f32_16x16x32_bf16(a, b, c, 0, 0, 0)

__device__ __forceinline__ void pf_line(unsigned voff, const void* sbase, unsigned lds) {
  unsigned keep;
  lds = __builtin_amdgcn_readfirstlane(lds);
  asm volatile("s_mov_b32 %0, m0\n\ts_mov_b32 m0, %3\n\ts_nop 0\n\tglobal_load_lds_dword %1, %2\n\ts_mov_b32 m0, %0"
               : "=&s"(keep) : "v"(voff), "s"(sbase), "s"(lds) : "memory");
}
__device__ __forceinline__ void glds16_sc1(unsigned voff, const void* sbase, unsigned lds) {
  unsigned keep;
  lds = __builtin_amdgcn_readfirstlane(lds);
  asm volatile("s_mov_b32 %0, m0\n\ts_mov_b32 m0, %3\n\ts_nop 0\n\tglobal_load_lds_dwordx4 %1, %2 sc1\n\ts_mov_b32 m0, %0"
               : "=&s"(keep) : "v"(voff), "s"(sbase), "s"(lds) : "memory");
}
__device__ __forceinline__ void st16_sc1(void* p, u32x4 v) {
  asm volatile("global_store_dwordx4 %0, %1, off sc1\n\ts_nop 1" ::"v"(p), "v"(v) : "memory");
}
typedef AS1 unsigned gu32;
constexpr int FLAG_STRIDE = 32;

__device__ __forceinline__ unsigned src_off_ks(int ins, int lane, int ld) {
  int r = ins * 8 + (lane >> 3);
  int h = ((r >> 1) & 1) | (((r >> 3) & 1) << 1);
  int ch = (lane & 7) ^ (2 * h);
  return (unsigned)(r * ld + ch * 8);
}
__device__ __forceinline__ unsigned src_off_kc(int ins, int lane, int ld) {
  int r = ins * 8 + (lane >> 3);
  int ch = (lane & 7) ^ ((r >> 1) & 7);
  return (unsigned)(r * ld + ch * 8);
}
__device__ __forceinline__ int frag_off_ks(int lane, int ct) {
  int g = lane >> 4, q = (lane & 15) >> 2, p = lane & 3;
  int h = ((q >> 1) & 1) | ((g & 1) << 1);
  return (8 * g + q) * 128 + (((ct ^ h) * 2 + (p >> 1)) * 16) + 8 * (p & 1);
}
__device__ __forceinline__ int frag_off_kc(int lane, int ks) {
  int g = lane >> 4, l15 = lane & 15;
  return l15 * 128 + (((ks * 4 + g) ^ (l15 >> 1)) * 16);
}

__device__ __forceinline__ float gelu_tanh(float v) {
  float z = 0.7978845608028654f * (v + 0.044715f * v * v * v);
  return v * __builtin_amdgcn_rcpf(1.f + __expf(-2.f * z));
}

constexpr int K1_STAGE = 34816;
constexpr int K1_NST = 4;
#define MFMA16(a, b, c) __builtin_amdgcn_mfma_f32_16x16x16bf16_1k(a, b, c, 0, 0, 0)
__device__ __forceinline__ s16x4 pack4(f32x4 v) {
  u32x2 t = {pack2(v[0], v[1]), pack2(v[2], v[3])};
  return __builtin_bit_cast(s16x4, t);
}
__global__ __launch_bounds__(512, 2) void k1_qkv_attn_ln(const u16* __restrict__ x, const u16* __restrict__ wqkv,
                                                         const u16* __restrict__ gate_w, u16* __restrict__ o,
                                                         u16* __restrict__ hbuf, float* __restrict__ gbuf,
                                                         const u16* __restrict__ w1, const u16* __restrict__ wo,
                                                         unsigned* __restrict__ flags) {
  __shared__ __attribute__((aligned(16))) char smem[K1_STAGE * K1_NST + 256];
  const int tid = threadIdx.x, lane = tid & 63;
  const int wv = __builtin_amdgcn_readfirstlane(tid >> 6);
  const int w = wv & 3;
  const int b = blockIdx.x, xcd = b & 7, idx = b >> 3;
  const int rt = (xcd >> 1) * 8 + (idx & 7), hg = (xcd & 1) * 4 + (idx >> 3);
  const int s0 = rt * 64;
  const int g = lane >> 4, l15 = lane & 15;
  const unsigned lbase = lds_addr(smem);

  if (wv >= 4) {
    unsigned gw[8];
#pragma unroll
    for (int i = 0; i < 8; ++i) gw[i] = *(const unsigned*)(gate_w + i * 64 + (lane & 31) * 2);
    unsigned gp[9];
#pragma unroll
    for (int m = 0; m < 9; ++m) {
      int j = w + 4 * m;
      if (j < 24) {
        gp[m] = ((j >> 3) * D_ + hg * 64 + src_off_ks(j & 7, lane, 3 * D_)) * 2u;
      } else {
        int jj = j - 24;
        jj = jj > 9 ? 9 : jj;
        int r = jj * 8 + (lane >> 3);
        int ch = (lane & 7) ^ ((r >> 1) & 7);
        int srow = s0 - 16 + r;
        srow = srow < 0 ? 0 : srow;
        gp[m] = (unsigned)(srow * D_ + ch * 8) * 2u;
      }
    }
    auto issue = [&](int st) {
      const unsigned dst = lbase + (st % K1_NST) * K1_STAGE + w * 1024;
      const u16* xa = x + st * 64;
      const u16* wa = wqkv + (size_t)st * 64 * 3 * D_;
      glds16(gp[6], xa, dst + 6 * 4096);
      glds16(gp[7], xa, dst + 7 * 4096);
      if (w < 2) glds16(gp[8], xa, dst + 8 * 4096);
#pragma unroll
      for (int m = 0; m < 6; ++m) glds16(gp[m], wa, dst + m * 4096);
    };
    const int lnr = 16 + hg * 8 + 2 * w + (lane >> 5);
    const int lnoff = 24576 + lnr * 128 + ((((lane & 31) >> 2) ^ ((lnr >> 1) & 7)) * 16) + (lane & 3) * 4;
    unsigned xs[8];
    issue(0); issue(1);
#pragma unroll
    for (int it = 0; it < 8; ++it) {
      constexpr int NI[9] = {2, 4, 5, 6, 7, 8, 8, 8, 8};
      const int ahead = NI[it] - it - 1;
      WAIT_L0();
      if (w < 2) { WAIT_V(ahead * 9); } else { WAIT_V(ahead * 8); }
      BAR();
#pragma unroll
      for (int st = NI[it]; st < NI[it + 1]; ++st) issue(st);
      xs[it] = *(const AS3 unsigned*)(smem + (it % K1_NST) * K1_STAGE + lnoff);
    }
    {
      float xv[16];
      float sum = 0.f, gs = 0.f;
#pragma unroll
      for (int i = 0; i < 8; ++i) {
        xv[2 * i] = lo16(xs[i]);
        xv[2 * i + 1] = hi16(xs[i]);
        sum += xv[2 * i] + xv[2 * i + 1];
        gs += xv[2 * i] * lo16(gw[i]) + xv[2 * i + 1] * hi16(gw[i]);
      }
#pragma unroll
      for (int m = 1; m < 32; m <<= 1) { sum += __shfl_xor(sum, m); gs += __shfl_xor(gs, m); }
      float mean = sum * (1.f / 512.f);
      float vs = 0.f;
#pragma unroll
      for (int i = 0; i < 16; ++i) { float d = xv[i] - mean; vs += d * d; }
#pragma unroll
      for (int m = 1; m < 32; m <<= 1) vs += __shfl_xor(vs, m);
      float rden = 1.f / sqrtf(vs * (1.f / 512.f) + 1e-5f);
      const int srow = s0 + hg * 8 + 2 * w + (lane >> 5);
#pragma unroll
      for (int i = 0; i < 8; ++i) {
        float a0 = (xv[2 * i] - mean) * rden, a1 = (xv[2 * i + 1] - mean) * rden;
        *(unsigned*)(hbuf + (size_t)srow * D_ + i * 64 + (lane & 31) * 2) = pack2(a0, a1);
      }
      if ((lane & 31) == 0) gbuf[srow] = 1.f / (1.f + __expf(-gs));
    }
  } else {
    const int offw = frag_off_ks(lane, w);
    int xoff[2];
#pragma unroll
    for (int ks = 0; ks < 2; ++ks) xoff[ks] = 24576 + frag_off_kc(lane, ks);
    f32x4 aq[4], ak[5], av[5];
#pragma unroll
    for (int i = 0; i < 5; ++i) { ak[i] = f32x4{0.f, 0.f, 0.f, 0.f}; av[i] = f32x4{0.f, 0.f, 0.f, 0.f}; if (i < 4) aq[i] = f32x4{0.f, 0.f, 0.f, 0.f}; }
#pragma unroll 1
    for (int it = 0; it < 8; ++it) {
      WAIT_L0();
      BAR();
      if (it == 2) {
        const unsigned dmy = lbase + K1_STAGE * K1_NST;
        const unsigned ro = (unsigned)((idx * 64 + lane) * F_ + (xcd >> 1) * 448) * 2u;
        pf_line(ro + w * 128, w1, dmy);
        if (w < 3) pf_line(ro + (w + 4) * 128, w1, dmy);
        if (w >= 2) pf_line((unsigned)(idx * 128 + (w - 2) * 64 + lane) * 128u, wo, dmy);
        if (b == 0 && tid < 40) flags[tid * FLAG_STRIDE] = 0u;
      }
      const char* sb = smem + (it % K1_NST) * K1_STAGE;
#pragma unroll
      for (int ks = 0; ks < 2; ++ks) {
        bf16x8 wq = ld_tr2(sb + offw + ks * 4096, 512);
        bf16x8 wk = ld_tr2(sb + 8192 + offw + ks * 4096, 512);
        bf16x8 wvv = ld_tr2(sb + 16384 + offw + ks * 4096, 512);
#pragma unroll
        for (int r = 0; r < 5; ++r) {
          bf16x8 xf = ld_b128(sb + xoff[ks] + r * 2048);
          if (r > 0) aq[r - 1] = MFMA(wq, xf, aq[r - 1]);
          ak[r] = MFMA(wk, xf, ak[r]);
          av[r] = MFMA(xf, wvv, av[r]);
        }
      }
    }
    s16x4 kb[5], vb[5];
#pragma unroll
    for (int r = 0; r < 5; ++r) { kb[r] = pack4(ak[r]); vb[r] = pack4(av[r]); }
#pragma unroll
    for (int t = 0; t < 4; ++t) {
      s16x4 qb = pack4(aq[t]);
      f32x4 z = {0.f, 0.f, 0.f, 0.f};
      f32x4 sp = MFMA16(kb[t], qb, z);
      f32x4 sc = MFMA16(kb[t + 1], qb, z);
      float lg[8];
      float mx = -3.0e38f;
#pragma unroll
      for (int j = 0; j < 4; ++j) {
        const int dp = l15 + 16 - (4 * g + j), dc = l15 - (4 * g + j);
        const bool vp = (dp <= 14) && !(dp & 1) && (s0 + t * 16 + l15 - dp >= 0);
        const bool vc = (dc >= 0) && (dc <= 14) && !(dc & 1);
        lg[j] = vp ? sp[j] * 0.25f : -3.0e38f;
        lg[4 + j] = vc ? sc[j] * 0.25f : -3.0e38f;
        mx = fmaxf(mx, fmaxf(lg[j], lg[4 + j]));
      }
      mx = fmaxf(mx, __shfl_xor(mx, 16));
      mx = fmaxf(mx, __shfl_xor(mx, 32));
      float sum = 0.f;
#pragma unroll
      for (int j = 0; j < 8; ++j) { lg[j] = __expf(lg[j] - mx); sum += lg[j]; }
      sum += __shfl_xor(sum, 16);
      sum += __shfl_xor(sum, 32);
      const float rs = 1.f / sum;
      s16x4 pp = pack4(f32x4{lg[0] * rs, lg[1] * rs, lg[2] * rs, lg[3] * rs});
      s16x4 pc = pack4(f32x4{lg[4] * rs, lg[5] * rs, lg[6] * rs, lg[7] * rs});
      f32x4 ot = MFMA16(vb[t], pp, z);
      ot = MFMA16(vb[t + 1], pc, ot);
      u32x2 v = {pack2(ot[0], ot[1]), pack2(ot[2], ot[3])};
      *(u32x2*)(o + (size_t)(s0 + t * 16 + l15) * D_ + (hg * 4 + w) * 16 + 4 * g) = v;
    }
  }
}

constexpr int NST = 9;

__device__ constexpr int n_issued(int j, int NK) {
  int a = 5 + 2 * j, c = (j - 1 + NST) > NST ? (j - 1 + NST) : NST;
  int m = a < c ? a : c;
  return m < NK ? m : NK;
}
__device__ constexpr int allowed(int done, int n, bool split) {
  int c = 0;
  for (int s = done + 1; s < n; ++s) c += (split && s < NST) ? 2 : 4;
  return c;
}

template <int NK, bool SPLIT, class ISSUE>
__device__ __forceinline__ void loader_stream(ISSUE issue) {
  issue(0, 0, SPLIT); issue(1, 1, SPLIT); issue(2, 2, SPLIT);
  WAIT_V(allowed(0, 3, SPLIT));
  BAR();
  constexpr int UN = (NST - 2) < (NK - 1) ? (NST - 2) : (NK - 1);
#pragma unroll
  for (int j = 0; j < UN; ++j) {
#pragma unroll
    for (int st = n_issued(j - 1, NK); st < n_issued(j, NK); ++st) issue(st, st % NST, SPLIT && st < NST);
    WAIT_V(allowed(j + 1, n_issued(j, NK), SPLIT));
    BAR();
  }
  if constexpr (NK > NST) {
    int buf = (UN - 2) % NST;
#pragma unroll 1
    for (int it = UN; it < NK - NST + 2; ++it) {
      issue(it - 2 + NST, buf, false);
      buf = buf + 1 == NST ? 0 : buf + 1;
      WAIT_V((NST - 3) * 4);
      BAR();
    }
#pragma unroll
    for (int t = 0; t < NST - 3; ++t) {
      WAIT_V((NST - 4 - t) * 4);
      BAR();
    }
  }
}

__global__ __launch_bounds__(512, 2) void k2_mlp(const u16* __restrict__ hbuf, const u16* __restrict__ w1,
                                                 const u16* __restrict__ b1, u16* ubuf,
                                                 const u16* __restrict__ o, const u16* __restrict__ wo,
                                                 const u16* __restrict__ w2,
                                                 const u16* __restrict__ b2, const float* __restrict__ gbuf,
                                                 u16* __restrict__ out, unsigned* flags) {
  __shared__ __attribute__((aligned(16))) char smem[NST * 16384 + 8192 + 512];
  const int tid = threadIdx.x, lane = tid & 63;
  const int wv = __builtin_amdgcn_readfirstlane(tid >> 6);
  const int w = wv & 3;
  const int b = blockIdx.x, xcd = b & 7, idx = b >> 3;
  const int g = lane >> 4, l15 = lane & 15;
  const unsigned lbase = lds_addr(smem);
  const int dt = (xcd & 1) * 4 + (idx & 3), ft = (xcd >> 1) * 7 + (idx >> 2);
  const int stl = xcd * 4 + (idx & 3), dt4 = idx >> 2;
  const int s0 = stl * 64, d0 = dt4 * 64;
  const int wr = w >> 1, wc = w & 1;
  AS3 unsigned* sync_words = (AS3 unsigned*)(smem + NST * 16384 + 8192 + 256);

  if (wv >= 4) {
    unsigned pw1[2], ph[2], pwo[2], po[2], pu[2], pw2[2];
#pragma unroll
    for (int i = 0; i < 2; ++i) {
      pw1[i] = (ft * 64 + src_off_ks(2 * w + i, lane, F_)) * 2u;
      ph[i] = (dt * 64 + src_off_ks(2 * w + i, lane, D_)) * 2u;
      pwo[i] = (d0 + src_off_ks(2 * w + i, lane, D_)) * 2u;
      po[i] = (s0 * D_ + src_off_kc(2 * w + i, lane, D_)) * 2u;
      pu[i] = (d0 * F_ + src_off_kc(2 * w + i, lane, F_)) * 2u;
      pw2[i] = (s0 + src_off_ks(2 * w + i, lane, S_)) * 2u;
    }
    auto issueB = [&](int st, int buf) {
      const unsigned dst = lbase + buf * 16384 + w * 2048;
      const u16* a0 = wo + (size_t)st * 64 * D_;
      const u16* a1 = o + st * 64;
      glds16(pwo[0], a0, dst);
      glds16(pwo[1], a0, dst + 1024);
      glds16(po[0], a1, dst + 8192);
      glds16(po[1], a1, dst + 8192 + 1024);
    };
    if (b < 224) {
      loader_stream<40, false>([&](int st, int buf, bool) {
        if (st < 32) {
          const unsigned dst = lbase + buf * 16384 + w * 2048;
          const u16* a0 = w1 + (size_t)st * 64 * F_;
          const u16* a1 = hbuf + (size_t)st * 64 * D_;
          glds16(pw1[0], a0, dst);
          glds16(pw1[1], a0, dst + 1024);
          glds16(ph[0], a1, dst + 8192);
          glds16(ph[1], a1, dst + 8192 + 1024);
        } else {
          issueB(st - 32, buf);
        }
      });
    } else {
      loader_stream<8, false>([&](int st, int buf, bool) { issueB(st, buf); });
    }
    BAR();
#pragma unroll
    for (int st = 0; st < NST; ++st) {
      const unsigned dst = lbase + st * 16384 + w * 2048;
      const u16* a0 = w2 + (size_t)st * 64 * S_;
      glds16(pw2[0], a0, dst);
      glds16(pw2[1], a0, dst + 1024);
    }
    BAR();
    loader_stream<F_ / 64, true>([&](int st, int buf, bool only1) {
      const unsigned dst = lbase + buf * 16384 + w * 2048;
      const u16* a0 = w2 + (size_t)st * 64 * S_;
      const u16* a1 = ubuf + st * 64;
      if (!only1) {
        glds16(pw2[0], a0, dst);
        glds16(pw2[1], a0, dst + 1024);
      }
      glds16_sc1(pu[0], a1, dst + 8192);
      glds16_sc1(pu[1], a1, dst + 8192 + 1024);
    });
  } else {
    if (tid < 2) sync_words[tid] = 0u;
    float gv[2], b2v[2];
#pragma unroll
    for (int j = 0; j < 2; ++j) {
      int s = s0 + wc * 32 + j * 16 + l15;
      gv[j] = gbuf[s];
      b2v[j] = lo16((unsigned)b2[s]);
    }
    int off_ks_r[2], off_ks_c[2], off_kc[2];
#pragma unroll
    for (int i = 0; i < 2; ++i) { off_ks_r[i] = frag_off_ks(lane, wr * 2 + i); off_ks_c[i] = frag_off_ks(lane, wc * 2 + i); off_kc[i] = frag_off_kc(lane, i); }
    f32x4 acc[2][2];
    bf16x8 fa[2][2][2], fb[2][2][2];
    auto zero_acc = [&]() {
#pragma unroll
      for (int i = 0; i < 2; ++i)
#pragma unroll
        for (int j = 0; j < 2; ++j) acc[i][j] = f32x4{0.f, 0.f, 0.f, 0.f};
    };
    auto mm = [&](int set) {
#pragma unroll
      for (int ks = 0; ks < 2; ++ks)
#pragma unroll
        for (int i = 0; i < 2; ++i)
#pragma unroll
          for (int j = 0; j < 2; ++j) acc[i][j] = MFMA(fa[set][ks][i], fb[set][ks][j], acc[i][j]);
    };
    int buf = 0;
    auto nextbuf = [&]() { buf = buf + 1 == NST ? 0 : buf + 1; };
    if (b < 224) {
      const int dd0 = dt * 64, f0 = ft * 64;
      constexpr int NK = S_ / 64;
      u32x2 bb[2];
#pragma unroll
      for (int i = 0; i < 2; ++i) bb[i] = *(const u32x2*)(b1 + f0 + wr * 32 + i * 16 + 4 * g);
      zero_acc();
      auto rd = [&](int set) {
        const char* sb = smem + buf * 16384;
#pragma unroll
        for (int ks = 0; ks < 2; ++ks)
#pragma unroll
          for (int i = 0; i < 2; ++i) { fa[set][ks][i] = ld_tr2(sb + off_ks_r[i] + ks * 4096, 512); fb[set][ks][i] = ld_tr2(sb + 8192 + off_ks_c[i] + ks * 4096, 512); }
        nextbuf();
      };
      BAR();
      rd(0);
#pragma unroll 1
      for (int it = 0; it < NK - 2; it += 2) {
        BAR();
        if (it == 4) {
          if (lane < 56) pf_line((unsigned)((idx * 56 + lane) * S_ + xcd * 256 + w * 64) * 2u, w2, lbase + NST * 16384 + 8192);
        }
        rd(1);
        mm(0);
        BAR();
        rd(0);
        mm(1);
      }
      BAR();
      rd(1);
      mm(0);
      mm(1);
      char* eb = smem + NST * 16384;
#pragma unroll
      for (int i = 0; i < 2; ++i) {
        float bv[4] = {lo16(bb[i][0]), hi16(bb[i][0]), lo16(bb[i][1]), hi16(bb[i][1])};
#pragma unroll
        for (int j = 0; j < 2; ++j) {
          float r[4];
#pragma unroll
          for (int e = 0; e < 4; ++e) r[e] = gelu_tanh(acc[i][j][e] + bv[e]);
          u32x2 v = {pack2(r[0], r[1]), pack2(r[2], r[3])};
          const int row = wc * 32 + j * 16 + l15, ch = wr * 4 + i * 2 + (g >> 1);
          *(AS3 u32x2*)(eb + row * 128 + ((ch ^ (row & 7)) * 16) + (g & 1) * 8) = v;
        }
      }
      WAIT_L0();
      if (lane == 0) __hip_atomic_fetch_add(sync_words, 1u, __ATOMIC_RELAXED, __HIP_MEMORY_SCOPE_WORKGROUP);
      while (__hip_atomic_load(sync_words, __ATOMIC_RELAXED, __HIP_MEMORY_SCOPE_WORKGROUP) < 4u) {}
      asm volatile("" ::: "memory");
#pragma unroll
      for (int i = 0; i < 2; ++i) {
        const int row = w * 16 + i * 8 + (lane >> 3), ch = lane & 7;
        u32x4 v = *(const AS3 u32x4*)(eb + row * 128 + ((ch ^ (row & 7)) * 16));
        st16_sc1(ubuf + (size_t)(dd0 + row) * F_ + f0 + ch * 8, v);
      }
      WAIT_V(0);
      if (lane == 0) {
        unsigned old = __hip_atomic_fetch_add(sync_words + 1, 1u, __ATOMIC_RELAXED, __HIP_MEMORY_SCOPE_WORKGROUP);
        if (old == 3u) __hip_atomic_fetch_add((gu32*)(flags + dt * FLAG_STRIDE), 1u, __ATOMIC_RELAXED, __HIP_MEMORY_SCOPE_AGENT);
      }
    } else {
      if (lane < 56) pf_line((unsigned)((idx * 56 + lane) * S_ + xcd * 256 + w * 64) * 2u, w2, lbase + NST * 16384 + 8192);
    }
    f32x4 acca[2][2];
    {
      zero_acc();
      auto rd = [&](int set) {
        const char* sb = smem + buf * 16384;
#pragma unroll
        for (int ks = 0; ks < 2; ++ks)
#pragma unroll
          for (int i = 0; i < 2; ++i) { fa[set][ks][i] = ld_tr2(sb + off_ks_r[i] + ks * 4096, 512); fb[set][ks][i] = ld_b128(sb + 8192 + wc * 4096 + off_kc[ks] + i * 2048); }
        nextbuf();
      };
      BAR();
      rd(0);
#pragma unroll 1
      for (int it = 0; it < 6; it += 2) {
        BAR();
        rd(1);
        mm(0);
        BAR();
        rd(0);
        mm(1);
      }
      BAR();
      rd(1);
      mm(0);
      mm(1);
#pragma unroll
      for (int i = 0; i < 2; ++i)
#pragma unroll
        for (int j = 0; j < 2; ++j) acca[i][j] = acc[i][j];
    }
    WAIT_L0();
    BAR();
    if (wv == 0) {
      gu32* fu = (gu32*)(flags + dt4 * FLAG_STRIDE);
      for (unsigned spin = 0; spin < (1u << 24); ++spin) {
        unsigned vu = __hip_atomic_load(fu, __ATOMIC_RELAXED, __HIP_MEMORY_SCOPE_AGENT);
        if (vu >= 28u) break;
        __builtin_amdgcn_s_sleep(2);
      }
    }
    BAR();
    {
      constexpr int NK = F_ / 64;
      zero_acc();
      buf = 0;
      auto rd = [&](int set) {
        const char* sb = smem + buf * 16384;
#pragma unroll
        for (int ks = 0; ks < 2; ++ks)
#pragma unroll
          for (int i = 0; i < 2; ++i) { fa[set][ks][i] = ld_b128(sb + 8192 + wr * 4096 + off_kc[ks] + i * 2048); fb[set][ks][i] = ld_tr2(sb + off_ks_c[i] + ks * 4096, 512); }
        nextbuf();
      };
      BAR();
      rd(0);
#pragma unroll 1
      for (int it = 0; it < NK - 2; it += 2) {
        BAR();
        rd(1);
        mm(0);
        BAR();
        rd(0);
        mm(1);
      }
      BAR();
      rd(1);
      mm(0);
      mm(1);
    }
#pragma unroll
    for (int j = 0; j < 2; ++j) {
      const int s = s0 + wc * 32 + j * 16 + l15;
      const float gg = gv[j], og = 1.f - gg;
#pragma unroll
      for (int i = 0; i < 2; ++i) {
        float r[4];
#pragma unroll
        for (int e = 0; e < 4; ++e) r[e] = gg * acca[i][j][e] + og * (acc[i][j][e] + b2v[j]);
        u32x2 v = {pack2(r[0], r[1]), pack2(r[2], r[3])};
        *(u32x2*)(out + (size_t)s * D_ + d0 + wr * 32 + i * 16 + 4 * g) = v;
      }
    }
  }
}

extern "C" void kernel_launch(void* const* d_in, const int* in_sizes, int n_in, void* d_out, int out_size, void* d_ws,
                              size_t ws_size, hipStream_t stream) {
  const u16* x = (const u16*)d_in[0];
  const u16* wqkv = (const u16*)d_in[1];
  const u16* wo = (const u16*)d_in[2];
  const u16* w1 = (const u16*)d_in[4];
  const u16* b1 = (const u16*)d_in[5];
  const u16* w2 = (const u16*)d_in[6];
  const u16* b2 = (const u16*)d_in[7];
  const u16* gate_w = (const u16*)d_in[8];
  char* ws = (char*)d_ws;
  u16* obuf = (u16*)(ws);
  u16* hbuf = (u16*)(ws + (2 << 20));
  u16* ubuf = (u16*)(ws + (6 << 20));
  float* gbuf = (float*)(ws + (8 << 20));
  unsigned* flags = (unsigned*)(ws + (9 << 20));
  k1_qkv_attn_ln<<<256, 512, 0, stream>>>(x, wqkv, gate_w, obuf, hbuf, gbuf, w1, wo, flags);
  k2_mlp<<<256, 512, 0, stream>>>(hbuf, w1, b1, ubuf, obuf, wo, w2, b2, gbuf, (u16*)d_out, flags);
}
```

```cpp
#include <hip/hip_runtime.h>
#include <hip/hip_bf16.h>
#include <stdint.h>

typedef __attribute__((ext_vector_type(8))) short bf16x8;
typedef __attribute__((ext_vector_type(4))) short s16x4;
typedef __attribute__((ext_vector_type(4))) float f32x4;
typedef __attribute__((ext_vector_type(4))) unsigned u32x4;
typedef __attribute__((ext_vector_type(2))) unsigned u32x2;
typedef __attribute__((ext_vector_type(2))) __bf16 bf2;
typedef unsigned short u16;

#define AS1 __attribute__((address_space(1)))
#define AS3 __attribute__((address_space(3)))

constexpr int S_ = 2048, D_ = 512, F_ = 1792;

__device__ __forceinline__ void glds16(unsigned voff, const void* sbase, unsigned lds) {
  unsigned keep;
  lds = __builtin_amdgcn_readfirstlane(lds);
  asm volatile("s_mov_b32 %0, m0\n\ts_mov_b32 m0, %3\n\ts_nop 0\n\tglobal_load_lds_dwordx4 %1, %2\n\ts_mov_b32 m0, %0"
               : "=&s"(keep) : "v"(voff), "s"(sbase), "s"(lds) : "memory");
}
__device__ __forceinline__ unsigned lds_addr(const char* p) { return (unsigned)(size_t)(const AS3 char*)p; }
#define WAIT_V(n) asm volatile("s_waitcnt vmcnt(%0)" ::"n"(n) : "memory")
#define WAIT_L0() asm volatile("s_waitcnt lgkmcnt(0)" ::: "memory")
#define BAR() asm volatile("s_barrier" ::: "memory")

__device__ __forceinline__ unsigned pack2(float a, float b) {
  typedef __attribute__((ext_vector_type(2))) float f32x2;
  f32x2 t = {a, b};
  return __builtin_bit_cast(unsigned, __builtin_convertvector(t, bf2));
}
__device__ __forceinline__ float rbf(float a) { return (float)(__bf16)a; }
__device__ __forceinline__ float lo16(unsigned u) { return __builtin_bit_cast(float, u << 16); }
__device__ __forceinline__ float hi16(unsigned u) { return __builtin_bit_cast(float, u & 0xffff0000u); }

__device__ __forceinline__ bf16x8 ld_b128(const char* p) { return *(const AS3 bf16x8*)(p); }
__device__ __forceinline__ bf16x8 ld_tr2(const char* p, int t_off) {
  s16x4 lo = __builtin_amdgcn_ds_read_tr16_b64_v4i16((AS3 s16x4*)(p));
  s16x4 hi = __builtin_amdgcn_ds_read_tr16_b64_v4i16((AS3 s16x4*)(p + t_off));
  return __builtin_shufflevector(lo, hi, 0, 1, 2, 3, 4, 5, 6, 7);
}
#define MFMA(a, b, c) __builtin_amdgcn_mfma_f32_16x16x32_bf16(a, b, c, 0, 0, 0)

__device__ __forceinline__ void pf_line(unsigned voff, const void* sbase, unsigned lds) {
  unsigned keep;
  lds = __builtin_amdgcn_readfirstlane(lds);
  asm volatile("s_mov_b32 %0, m0\n\ts_mov_b32 m0, %3\n\ts_nop 0\n\tglobal_load_lds_dword %1, %2\n\ts_mov_b32 m0, %0"
               : "=&s"(keep) : "v"(voff), "s"(sbase), "s"(lds) : "memory");
}
__device__ __forceinline__ void glds16_sc1(unsigned voff, const void* sbase, unsigned lds) {
  unsigned keep;
  lds = __builtin_amdgcn_readfirstlane(lds);
  asm volatile("s_mov_b32 %0, m0\n\ts_mov_b32 m0, %3\n\ts_nop 0\n\tglobal_load_lds_dwordx4 %1, %2 sc1\n\ts_mov_b32 m0, %0"
               : "=&s"(keep) : "v"(voff), "s"(sbase), "s"(lds) : "memory");
}
__device__ __forceinline__ void st16_sc1(void* p, u32x4 v) {
  asm volatile("global_store_dwordx4 %0, %1, off sc1\n\ts_nop 1" ::"v"(p), "v"(v) : "memory");
}
typedef AS1 unsigned gu32;
constexpr int FLAG_STRIDE = 32;

__device__ __forceinline__ unsigned src_off_ks(int ins, int lane, int ld) {
  int r = ins * 8 + (lane >> 3);
  int h = ((r >> 1) & 1) | (((r >> 3) & 1) << 1);
  int ch = (lane & 7) ^ (2 * h);
  return (unsigned)(r * ld + ch * 8);
}
__device__ __forceinline__ unsigned src_off_kc(int ins, int lane, int ld) {
  int r = ins * 8 + (lane >> 3);
  int ch = (lane & 7) ^ ((r >> 1) & 7);
  return (unsigned)(r * ld + ch * 8);
}
__device__ __forceinline__ int frag_off_ks(int lane, int ct) {
  int g = lane >> 4, q = (lane & 15) >> 2, p = lane & 3;
  int h = ((q >> 1) & 1) | ((g & 1) << 1);
  return (8 * g + q) * 128 + (((ct ^ h) * 2 + (p >> 1)) * 16) + 8 * (p & 1);
}
__device__ __forceinline__ int frag_off_kc(int lane, int ks) {
  int g = lane >> 4, l15 = lane & 15;
  return l15 * 128 + (((ks * 4 + g) ^ (l15 >> 1)) * 16);
}

template <int CTRL> __device__ __forceinline__ float dpp_f(float v) {
  return __builtin_bit_cast(float, __builtin_amdgcn_update_dpp(0, __builtin_bit_cast(int, v), CTRL, 0xf, 0xf, true));
}
__device__ __forceinline__ void sw16(float v, float& a, float& b) {
  unsigned u = __builtin_bit_cast(unsigned, v);
  auto r = __builtin_amdgcn_permlane16_swap(u, u, false, false);
  a = __builtin_bit_cast(float, (unsigned)r[0]); b = __builtin_bit_cast(float, (unsigned)r[1]);
}
__device__ __forceinline__ void sw32(float v, float& a, float& b) {
  unsigned u = __builtin_bit_cast(unsigned, v);
  auto r = __builtin_amdgcn_permlane32_swap(u, u, false, false);
  a = __builtin_bit_cast(float, (unsigned)r[0]); b = __builtin_bit_cast(float, (unsigned)r[1]);
}
__device__ __forceinline__ float max3(float a, float b, float c) {
  float r;
  asm("v_max3_f32 %0, %1, %2, %3" : "=v"(r) : "v"(a), "v"(b), "v"(c));
  return r;
}
__device__ __forceinline__ float sum32(float v) {
  v += dpp_f<0xB1>(v);
  v += dpp_f<0x4E>(v);
  v += dpp_f<0x141>(v);
  v += dpp_f<0x140>(v);
  float a, b;
  sw16(v, a, b);
  return a + b;
}
#define SB0() __builtin_amdgcn_sched_barrier(0)
template <int N, int R, int M> __device__ __forceinline__ void interleave() {
#pragma unroll
  for (int i = 0; i < N; ++i) { __builtin_amdgcn_sched_group_barrier(0x008, 1, 0); __builtin_amdgcn_sched_group_barrier(0x100, R, 0); }
  if (M > 0) __builtin_amdgcn_sched_group_barrier(0x008, M, 0);
}
__device__ __forceinline__ float gelu_tanh(float v) {
  const float c1 = -2.f * 0.7978845608028654f * 1.4426950408889634f, c2 = c1 * 0.044715f;
  float e = __builtin_amdgcn_exp2f(v * __builtin_fmaf(v * v, c2, c1));
  return v * __builtin_amdgcn_rcpf(1.f + e);
}

constexpr int K1_STAGE = 34816;
constexpr int K1_NST = 4;
#define WOPF 0
#define K1PF 5
#define MFMA16(a, b, c) __builtin_amdgcn_mfma_f32_16x16x16bf16_1k(a, b, c, 0, 0, 0)
__device__ __forceinline__ s16x4 pack4(f32x4 v) {
  u32x2 t = {pack2(v[0], v[1]), pack2(v[2], v[3])};
  return __builtin_bit_cast(s16x4, t);
}
__global__ __launch_bounds__(512, 2) void k1_qkv_attn_ln(const u16* __restrict__ x, const u16* __restrict__ wqkv,
                                                         const u16* __restrict__ gate_w, u16* __restrict__ o,
                                                         u16* __restrict__ hbuf, float* __restrict__ gbuf,
                                                         const u16* __restrict__ w1, const u16* __restrict__ wo,
                                                         unsigned* __restrict__ flags) {
  __shared__ __attribute__((aligned(16))) char smem[K1_STAGE * K1_NST + 256];
  const int tid = threadIdx.x, lane = tid & 63;
  const int wv = __builtin_amdgcn_readfirstlane(tid >> 6);
  const int w = wv & 3;
  const int b = blockIdx.x, xcd = b & 7, idx = b >> 3;
  const int rt = (xcd >> 1) * 8 + (idx & 7), hg = (xcd & 1) * 4 + (idx >> 3);
  const int s0 = rt * 64;
  const int g = lane >> 4, l15 = lane & 15;
  const unsigned lbase = lds_addr(smem);
  const int rot = ((idx & 7) + 2 * (idx >> 3)) & 7;

  if (wv >= 4) {
    unsigned gw[8];
#pragma unroll
    for (int i = 0; i < 8; ++i) gw[i] = *(const unsigned*)(gate_w + ((i + rot) & 7) * 64 + (lane & 31) * 2);
    unsigned gp[9];
#pragma unroll
    for (int m = 0; m < 9; ++m) {
      int j = w + 4 * m;
      if (j < 24) {
        gp[m] = ((j >> 3) * D_ + hg * 64 + src_off_ks(j & 7, lane, 3 * D_)) * 2u;
      } else {
        int jj = j - 24;
        jj = jj > 9 ? 9 : jj;
        int r = jj * 8 + (lane >> 3);
        int ch = (lane & 7) ^ ((r >> 1) & 7);
        int srow = s0 - 16 + r;
        srow = srow < 0 ? 0 : srow;
        gp[m] = (unsigned)(srow * D_ + ch * 8) * 2u;
      }
    }
    auto issue = [&](int st) {
      const unsigned dst = lbase + (st % K1_NST) * K1_STAGE + w * 1024;
      const int sr = (st + rot) & 7;
      const u16* xa = x + sr * 64;
      const u16* wa = wqkv + (size_t)sr * 64 * 3 * D_;
      glds16(gp[6], xa, dst + 6 * 4096);
      glds16(gp[7], xa, dst + 7 * 4096);
      if (w < 2) glds16(gp[8], xa, dst + 8 * 4096);
#pragma unroll
      for (int m = 0; m < 6; ++m) glds16(gp[m], wa, dst + m * 4096);
    };
    const int lnr = 16 + hg * 8 + 2 * w + (lane >> 5);
    const int lnoff = 24576 + lnr * 128 + ((((lane & 31) >> 2) ^ ((lnr >> 1) & 7)) * 16) + (lane & 3) * 4;
    unsigned xs[8];
    issue(0); issue(1);
#pragma unroll
    for (int it = 0; it < 8; ++it) {
      constexpr int NI[9] = {2, 4, 5, 6, 7, 8, 8, 8, 8};
      const int ahead = NI[it] - it - 1;
      const int npf = it > K1PF ? 2 + WOPF : 0;
      WAIT_L0();
      if (w < 2) { WAIT_V(ahead * 9 + npf); } else { WAIT_V(ahead * 8 + npf); }
      BAR();
#pragma unroll
      for (int st = NI[it]; st < NI[it + 1]; ++st) issue(st);
      if (it == K1PF) {
        const unsigned dmy = lbase + K1_STAGE * K1_NST;
        const int ln = (lane & 7) < 7 ? (lane & 7) : 6;
        const unsigned ro = (unsigned)((idx * 64 + w * 16 + (lane >> 3)) * F_ + (xcd >> 1) * 448) * 2u + ln * 128;
#pragma unroll
        for (int i = 0; i < 2; ++i) pf_line(ro + i * 8 * F_ * 2, w1, dmy);
      }
      xs[it] = *(const AS3 unsigned*)(smem + (it % K1_NST) * K1_STAGE + lnoff);
    }
    {
      float xv[16];
      float sum = 0.f, gs = 0.f;
#pragma unroll
      for (int i = 0; i < 8; ++i) {
        xv[2 * i] = lo16(xs[i]);
        xv[2 * i + 1] = hi16(xs[i]);
        sum += xv[2 * i] + xv[2 * i + 1];
        gs += xv[2 * i] * lo16(gw[i]) + xv[2 * i + 1] * hi16(gw[i]);
      }
      sum = sum32(sum);
      gs = sum32(gs);
      float mean = sum * (1.f / 512.f);
      float vs = 0.f;
#pragma unroll
      for (int i = 0; i < 16; ++i) { float d = xv[i] - mean; vs += d * d; }
      vs = sum32(vs);
      float rden = 1.f / sqrtf(vs * (1.f / 512.f) + 1e-5f);
      const int srow = s0 + hg * 8 + 2 * w + (lane >> 5);
#pragma unroll
      for (int i = 0; i < 8; ++i) {
        float a0 = (xv[2 * i] - mean) * rden, a1 = (xv[2 * i + 1] - mean) * rden;
        *(unsigned*)(hbuf + (size_t)srow * D_ + ((i + rot) & 7) * 64 + (lane & 31) * 2) = pack2(a0, a1);
      }
      if ((lane & 31) == 0) gbuf[srow] = 1.f / (1.f + __expf(-gs));
    }
  } else {
    const int offw = frag_off_ks(lane, w);
    int xoff[2];
#pragma unroll
    for (int ks = 0; ks < 2; ++ks) xoff[ks] = 24576 + frag_off_kc(lane, ks);
    f32x4 aq[4], ak[5], av[5];
#pragma unroll
    for (int i = 0; i < 5; ++i) { ak[i] = f32x4{0.f, 0.f, 0.f, 0.f}; av[i] = f32x4{0.f, 0.f, 0.f, 0.f}; if (i < 4) aq[i] = f32x4{0.f, 0.f, 0.f, 0.f}; }
    bf16x8 fw[2][3], fx[2][5];
    auto rd = [&](int it, int ks) {
      const char* sb = smem + (it % K1_NST) * K1_STAGE;
#pragma unroll
      for (int m = 0; m < 3; ++m) fw[ks][m] = ld_tr2(sb + m * 8192 + offw + ks * 4096, 512);
#pragma unroll
      for (int r = 0; r < 5; ++r) fx[ks][r] = ld_b128(sb + xoff[ks] + r * 2048);
    };
    auto mm = [&](int ks) {
#pragma unroll
      for (int r = 0; r < 5; ++r) {
        if (r > 0) aq[r - 1] = MFMA(fw[ks][0], fx[ks][r], aq[r - 1]);
        ak[r] = MFMA(fw[ks][1], fx[ks][r], ak[r]);
        av[r] = MFMA(fx[ks][r], fw[ks][2], av[r]);
      }
    };
    BAR();
    if (b == 0) flags[tid] = 0u;
    rd(0, 0);
#pragma unroll 1
    for (int it = 0; it < 7; ++it) {
      SB0();
      rd(it, 1);
      mm(0);
      interleave<6, 2, 8>();
      SB0();
      WAIT_L0();
      BAR();
      SB0();
      rd(it + 1, 0);
      mm(1);
      interleave<6, 2, 8>();
    }
    SB0();
    rd(7, 1);
    mm(0);
    interleave<6, 2, 8>();
    SB0();
    mm(1);
    s16x4 kb[5], vb[5];
#pragma unroll
    for (int r = 0; r < 5; ++r) { kb[r] = pack4(ak[r]); vb[r] = pack4(av[r]); }
#pragma unroll
    for (int t = 0; t < 4; ++t) {
      s16x4 qb = pack4(aq[t]);
      f32x4 z = {0.f, 0.f, 0.f, 0.f};
      f32x4 sp = MFMA16(kb[t], qb, z);
      f32x4 sc = MFMA16(kb[t + 1], qb, z);
      float lg[8];
      float mx = -3.0e38f;
#pragma unroll
      for (int j = 0; j < 4; ++j) {
        const int dp = l15 + 16 - (4 * g + j), dc = l15 - (4 * g + j);
        const bool vp = (dp <= 14) && !(dp & 1) && (s0 + t * 16 + l15 - dp >= 0);
        const bool vc = (dc >= 0) && (dc <= 14) && !(dc & 1);
        lg[j] = vp ? sp[j] : -3.0e38f;
        lg[4 + j] = vc ? sc[j] : -3.0e38f;
      }
      mx = max3(max3(max3(lg[0], lg[1], lg[2]), lg[3], lg[4]), max3(lg[5], lg[6], lg[7]), mx);
      { float a, c; sw16(mx, a, c); mx = max3(a, c, c); sw32(mx, a, c); mx = max3(a, c, c); }
      float sum = 0.f;
      const float cs = 0.25f * 1.4426950408889634f, mc = -mx * cs;
#pragma unroll
      for (int j = 0; j < 8; ++j) { lg[j] = __builtin_amdgcn_exp2f(__builtin_fmaf(lg[j], cs, mc)); sum += lg[j]; }
      { float a, c; sw16(sum, a, c); sum = a + c; sw32(sum, a, c); sum = a + c; }
      const float rs = __builtin_amdgcn_rcpf(sum);
      s16x4 pp = pack4(f32x4{lg[0] * rs, lg[1] * rs, lg[2] * rs, lg[3] * rs});
      s16x4 pc = pack4(f32x4{lg[4] * rs, lg[5] * rs, lg[6] * rs, lg[7] * rs});
      f32x4 ot = MFMA16(vb[t], pp, z);
      ot = MFMA16(vb[t + 1], pc, ot);
      u32x2 v = {pack2(ot[0], ot[1]), pack2(ot[2], ot[3])};
      *(u32x2*)(o + (size_t)(s0 + t * 16 + l15) * D_ + (hg * 4 + w) * 16 + 4 * g) = v;
    }
  }
}

constexpr int NST = 9;
#define EARLY 1
#define GLATE 0
#define SECT 0
#define OPF 1
#define EPOLL 1
#define PSLP 1
#define PUB_IT 0
#define ROTD 0

__device__ constexpr int n_issued(int j, int NK) {
  int a = 5 + 2 * j, c = (j - 1 + NST) > NST ? (j - 1 + NST) : NST;
  int m = a < c ? a : c;
  return m < NK ? m : NK;
}
__device__ constexpr int allowed(int done, int n, bool split) {
  int c = 0;
  for (int s = done + 1; s < n; ++s) c += (split && s < NST) ? 2 : 4;
  return c;
}

template <int NK, bool SPLIT, class ISSUE>
__device__ __forceinline__ void loader_stream(ISSUE issue) {
  issue(0, 0, SPLIT); issue(1, 1, SPLIT); issue(2, 2, SPLIT);
  WAIT_V(allowed(0, 3, SPLIT));
  BAR();
  constexpr int UN = (NST - 2) < (NK - 1) ? (NST - 2) : (NK - 1);
#pragma unroll
  for (int j = 0; j < UN; ++j) {
#pragma unroll
    for (int st = n_issued(j - 1, NK); st < n_issued(j, NK); ++st) issue(st, st % NST, SPLIT && st < NST);
    WAIT_V(allowed(j + 1, n_issued(j, NK), SPLIT));
    BAR();
  }
  if constexpr (NK > NST) {
    int buf = (UN - 2) % NST;
#pragma unroll 1
    for (int it = UN; it < NK - NST + 2; ++it) {
      issue(it - 2 + NST, buf, false);
      buf = buf + 1 == NST ? 0 : buf + 1;
      WAIT_V((NST - 3) * 4);
      BAR();
    }
#pragma unroll
    for (int t = 0; t < NST - 3; ++t) {
      WAIT_V((NST - 4 - t) * 4);
      BAR();
    }
  }
}

__global__ __launch_bounds__(512, 2) void k2_mlp(const u16* __restrict__ hbuf, const u16* __restrict__ w1,
                                                 const u16* __restrict__ b1, u16* ubuf,
                                                 const u16* __restrict__ o, const u16* __restrict__ wo,
                                                 const u16* __restrict__ w2,
                                                 const u16* __restrict__ b2, const float* __restrict__ gbuf,
                                                 u16* __restrict__ out, unsigned* flags) {
  __shared__ __attribute__((aligned(16))) char smem[NST * 16384 + 8192 + 512];
  const int tid = threadIdx.x, lane = tid & 63;
  const int wv = __builtin_amdgcn_readfirstlane(tid >> 6);
  const int w = wv & 3;
  const int b = blockIdx.x, xcd = b & 7, idx = b >> 3;
  const int g = lane >> 4, l15 = lane & 15;
  const unsigned lbase = lds_addr(smem);
  const int dt = (xcd & 1) * 4 + (idx & 3), ft = (xcd >> 1) * 7 + (idx >> 2);
  const int stl = xcd * 4 + (idx & 3), dt4 = idx >> 2;
  const int s0 = stl * 64, d0 = dt4 * 64;
  const int wr = w >> 1, wc = w & 1;
  AS3 unsigned* sync_words = (AS3 unsigned*)(smem + NST * 16384 + 8192 + 256);

  if (wv >= 4) {
    unsigned pw1[2], ph[2], pwo[2], po[2], pu[2], pw2[2];
#pragma unroll
    for (int i = 0; i < 2; ++i) {
      pw1[i] = (ft * 64 + src_off_ks(2 * w + i, lane, F_)) * 2u;
      ph[i] = (dt * 64 + src_off_ks(2 * w + i, lane, D_)) * 2u;
      pwo[i] = (d0 + src_off_ks(2 * w + i, lane, D_)) * 2u;
      po[i] = (s0 * D_ + src_off_kc(2 * w + i, lane, D_)) * 2u;
      pu[i] = (d0 * F_ + src_off_kc(2 * w + i, lane, F_)) * 2u;
      pw2[i] = (s0 + src_off_ks(2 * w + i, lane, S_)) * 2u;
    }
    const int rotA = (((idx >> 2) * 32) / 7 + ROTD * (idx & 3)) & 31;
    auto issueB = [&](int st, int buf) {
      const unsigned dst = lbase + buf * 16384 + w * 2048;
      const int sr = (st + dt4) & 7;
      const u16* a0 = wo + (size_t)sr * 64 * D_;
      const u16* a1 = o + sr * 64;
      glds16(pwo[0], a0, dst);
      glds16(pwo[1], a0, dst + 1024);
      glds16(po[0], a1, dst + 8192);
      glds16(po[1], a1, dst + 8192 + 1024);
    };
    if (b < 224) {
      loader_stream<40, false>([&](int st, int buf, bool) {
        if (st < 32) {
          const unsigned dst = lbase + buf * 16384 + w * 2048;
          const int sr = (st + rotA) & 31;
          const u16* a0 = w1 + (size_t)sr * 64 * F_;
          const u16* a1 = hbuf + (size_t)sr * 64 * D_;
          glds16(pw1[0], a0, dst);
          glds16(pw1[1], a0, dst + 1024);
          glds16(ph[0], a1, dst + 8192);
          glds16(ph[1], a1, dst + 8192 + 1024);
        } else {
          issueB(st - 32, buf);
        }
      });
    } else {
      loader_stream<8, false>([&](int st, int buf, bool) { issueB(st, buf); });
    }
    BAR();
#pragma unroll
    for (int st = 0; st < (EARLY ? NST : 0); ++st) {
      const unsigned dst = lbase + st * 16384 + w * 2048;
      const int sr = st;
      const u16* a0 = w2 + (size_t)sr * 64 * S_;
      glds16(pw2[0], a0, dst);
      glds16(pw2[1], a0, dst + 1024);
    }
    BAR();
    loader_stream<F_ / 64, EARLY>([&](int st, int buf, bool only1) {
      const unsigned dst = lbase + buf * 16384 + w * 2048;
      const int sr = st;
      const u16* a0 = w2 + (size_t)sr * 64 * S_;
      const u16* a1 = ubuf + sr * 64;
      if (!only1) {
        glds16(pw2[0], a0, dst);
        glds16(pw2[1], a0, dst + 1024);
      }
      glds16_sc1(pu[0], a1, dst + 8192);
      glds16_sc1(pu[1], a1, dst + 8192 + 1024);
    });
  } else {
    if (tid < 2) sync_words[tid] = 0u;
    int off_ks_r[2], off_ks_c[2], off_kc[2];
#pragma unroll
    for (int i = 0; i < 2; ++i) { off_ks_r[i] = frag_off_ks(lane, wr * 2 + i); off_ks_c[i] = frag_off_ks(lane, wc * 2 + i); off_kc[i] = frag_off_kc(lane, i); }
    f32x4 acc[2][2];
    bf16x8 fa[2][2][2], fb[2][2][2];
    auto zero_acc = [&]() {
#pragma unroll
      for (int i = 0; i < 2; ++i)
#pragma unroll
        for (int j = 0; j < 2; ++j) acc[i][j] = f32x4{0.f, 0.f, 0.f, 0.f};
    };
    auto mm = [&](int set) {
#pragma unroll
      for (int ks = 0; ks < 2; ++ks)
#pragma unroll
        for (int i = 0; i < 2; ++i)
#pragma unroll
          for (int j = 0; j < 2; ++j) acc[i][j] = MFMA(fa[set][ks][i], fb[set][ks][j], acc[i][j]);
    };
    int buf = 0;
    auto nextbuf = [&]() { buf = buf + 1 == NST ? 0 : buf + 1; };
    if (b < 224) {
      const int dd0 = dt * 64, f0 = ft * 64;
      constexpr int NK = S_ / 64;
      u32x2 bb[2];
      {
        const unsigned* bp = (const unsigned*)(b1 + f0 + wr * 32);
        unsigned t[16];
#pragma unroll
        for (int i = 0; i < 16; ++i) t[i] = bp[i];
#pragma unroll
        for (int i = 0; i < 2; ++i)
#pragma unroll
          for (int k = 0; k < 2; ++k) {
            unsigned lo = (g & 1) ? t[i * 8 + 2 + k] : t[i * 8 + k];
            unsigned hi = (g & 1) ? t[i * 8 + 6 + k] : t[i * 8 + 4 + k];
            bb[i][k] = (g & 2) ? hi : lo;
          }
      }
      zero_acc();
      auto rd = [&](int set) {
        const char* sb = smem + buf * 16384;
#pragma unroll
        for (int ks = 0; ks < 2; ++ks)
#pragma unroll
          for (int i = 0; i < 2; ++i) { fa[set][ks][i] = ld_tr2(sb + off_ks_r[i] + ks * 4096, 512); fb[set][ks][i] = ld_tr2(sb + 8192 + off_ks_c[i] + ks * 4096, 512); }
        nextbuf();
      };
      BAR();
      rd(0);
#pragma unroll 1
      for (int it = 0; it < NK - 2; it += 2) {
        BAR();
        rd(1);
        mm(0);
        BAR();
        rd(0);
        mm(1);
      }
      BAR();
      rd(1);
      mm(0);
      mm(1);
      {
        float bv[2][4];
#pragma unroll
        for (int i = 0; i < 2; ++i) { bv[i][0] = lo16(bb[i][0]); bv[i][1] = hi16(bb[i][0]); bv[i][2] = lo16(bb[i][1]); bv[i][3] = hi16(bb[i][1]); }
#pragma unroll
        for (int j = 0; j < 2; ++j) {
          unsigned p[2][2];
#pragma unroll
          for (int i = 0; i < 2; ++i) {
            float r[4];
#pragma unroll
            for (int e = 0; e < 4; ++e) r[e] = gelu_tanh(acc[i][j][e] + bv[i][e]);
            p[i][0] = pack2(r[0], r[1]); p[i][1] = pack2(r[2], r[3]);
          }
          auto q0 = __builtin_amdgcn_permlane16_swap(p[0][0], p[1][0], false, false);
          auto q1 = __builtin_amdgcn_permlane16_swap(p[0][1], p[1][1], false, false);
          u32x4 v = {(unsigned)q0[0], (unsigned)q1[0], (unsigned)q0[1], (unsigned)q1[1]};
          const int row = wc * 32 + j * 16 + l15;
          st16_sc1(ubuf + (size_t)(dd0 + row) * F_ + f0 + wr * 32 + (g & 1) * 16 + (g >> 1) * 8, v);
        }
      }
      if (PUB_IT < 0) {
        WAIT_V(0);
        if (lane == 0) {
          unsigned old = __hip_atomic_fetch_add(sync_words + 1, 1u, __ATOMIC_RELAXED, __HIP_MEMORY_SCOPE_WORKGROUP);
          if (old == 3u) __hip_atomic_store((gu32*)(flags + dt * FLAG_STRIDE + ft), 1u, __ATOMIC_RELAXED, __HIP_MEMORY_SCOPE_AGENT);
        }
      }
    } else {
      const unsigned dmy = lbase + NST * 16384 + 8192;
      const int wi = (idx - 28) * 4 + w;
      if (wi == 0) pf_line((unsigned)(xcd * 1024 + (lane & 7) * 128), gbuf, dmy);
      if (wi == 1) pf_line((unsigned)(xcd * 512 + (lane & 3) * 128), b2, dmy);
#pragma unroll
      for (int i = 0; i < 7; ++i)
        pf_line((unsigned)((i * 256 + wi * 16 + (lane >> 2)) * S_ + xcd * 256 + (lane & 3) * 64) * 2u, w2, dmy);
#pragma unroll
      for (int i = 0; i < 2; ++i) pf_line((unsigned)(xcd * 256 * D_ * 2 + (wi * 128 + i * 64 + lane) * 128), o, dmy);
    }
    f32x4 acca[2][2];
    gu32* fu = (gu32*)(flags + dt4 * FLAG_STRIDE + (lane < 28 ? lane : 27));
    unsigned vu = 0u;
    {
      zero_acc();
      auto rd = [&](int set) {
        const char* sb = smem + buf * 16384;
#pragma unroll
        for (int ks = 0; ks < 2; ++ks)
#pragma unroll
          for (int i = 0; i < 2; ++i) { fa[set][ks][i] = ld_tr2(sb + off_ks_r[i] + ks * 4096, 512); fb[set][ks][i] = ld_b128(sb + 8192 + wc * 4096 + off_kc[ks] + i * 2048); }
        nextbuf();
      };
      BAR();
      rd(0);
#pragma unroll 1
      for (int it = 0; it < 6; it += 2) {
        BAR();
        rd(1);
        mm(0);
        BAR();
        rd(0);
        if (it == PUB_IT && b < 224) {
          WAIT_V(0);
          if (lane == 0) {
            unsigned old = __hip_atomic_fetch_add(sync_words + 1, 1u, __ATOMIC_RELAXED, __HIP_MEMORY_SCOPE_WORKGROUP);
            if (old == 3u) __hip_atomic_store((gu32*)(flags + dt * FLAG_STRIDE + ft), 1u, __ATOMIC_RELAXED, __HIP_MEMORY_SCOPE_AGENT);
          }
        }
        mm(1);
      }
      if (wv == 0) vu = __hip_atomic_load(fu, __ATOMIC_RELAXED, __HIP_MEMORY_SCOPE_AGENT);
      BAR();
      rd(1);
      mm(0);
      mm(1);
#pragma unroll
      for (int i = 0; i < 2; ++i)
#pragma unroll
        for (int j = 0; j < 2; ++j) acca[i][j] = acc[i][j];
    }
    WAIT_L0();
    BAR();
    float gv[2], b2v[2];
#pragma unroll
    for (int j = 0; j < 2; ++j) {
      int s = s0 + wc * 32 + j * 16 + l15;
      gv[j] = gbuf[s];
      b2v[j] = lo16((unsigned)b2[s]);
    }
    if (wv == 0) {
      for (unsigned spin = 0; spin < (1u << 24); ++spin) {
        if (__builtin_amdgcn_ballot_w64(vu == 0u) == 0ull) break;
        __builtin_amdgcn_s_sleep(PSLP);
        vu = __hip_atomic_load(fu, __ATOMIC_RELAXED, __HIP_MEMORY_SCOPE_AGENT);
      }
    }
    BAR();
    {
      constexpr int NK = F_ / 64;
      zero_acc();
      buf = 0;
      auto rd = [&](int set) {
        const char* sb = smem + buf * 16384;
#pragma unroll
        for (int ks = 0; ks < 2; ++ks)
#pragma unroll
          for (int i = 0; i < 2; ++i) { fa[set][ks][i] = ld_b128(sb + 8192 + wr * 4096 + off_kc[ks] + i * 2048); fb[set][ks][i] = ld_tr2(sb + off_ks_c[i] + ks * 4096, 512); }
        nextbuf();
      };
      BAR();
      rd(0);
#pragma unroll 1
      for (int it = 0; it < NK - 2; it += 2) {
        BAR();
        rd(1);
        mm(0);
        BAR();
        rd(0);
        mm(1);
      }
      BAR();
      rd(1);
      mm(0);
      mm(1);
    }
#pragma unroll
    for (int j = 0; j < 2; ++j) {
      const int s = s0 + wc * 32 + j * 16 + l15;
      const float gg = gv[j], og = 1.f - gg;
#pragma unroll
      for (int i = 0; i < 2; ++i) {
        float r[4];
#pragma unroll
        for (int e = 0; e < 4; ++e) r[e] = gg * acca[i][j][e] + og * (acc[i][j][e] + b2v[j]);
        u32x2 v = {pack2(r[0], r[1]), pack2(r[2], r[3])};
        *(u32x2*)(out + (size_t)s * D_ + d0 + wr * 32 + i * 16 + 4 * g) = v;
      }
    }
  }
}

extern "C" void kernel_launch(void* const* d_in, const int* in_sizes, int n_in, void* d_out, int out_size, void* d_ws,
                              size_t ws_size, hipStream_t stream) {
  const u16* x = (const u16*)d_in[0];
  const u16* wqkv = (const u16*)d_in[1];
  const u16* wo = (const u16*)d_in[2];
  const u16* w1 = (const u16*)d_in[4];
  const u16* b1 = (const u16*)d_in[5];
  const u16* w2 = (const u16*)d_in[6];
  const u16* b2 = (const u16*)d_in[7];
  const u16* gate_w = (const u16*)d_in[8];
  char* ws = (char*)d_ws;
  u16* obuf = (u16*)(ws);
  u16* hbuf = (u16*)(ws + (2 << 20));
  u16* ubuf = (u16*)(ws + (6 << 20));
  float* gbuf = (float*)(ws + (8 << 20));
  unsigned* flags = (unsigned*)(ws + (9 << 20));
  k1_qkv_attn_ln<<<256, 512, 0, stream>>>(x, wqkv, gate_w, obuf, hbuf, gbuf, w1, wo, flags);
  k2_mlp<<<256, 512, 0, stream>>>(hbuf, w1, b1, ubuf, obuf, wo, w2, b2, gbuf, (u16*)d_out, flags);
}
```

```cpp
#include <hip/hip_runtime.h>
#include <hip/hip_bf16.h>
#include <stdint.h>

typedef __attribute__((ext_vector_type(8))) short bf16x8;
typedef __attribute__((ext_vector_type(4))) short s16x4;
typedef __attribute__((ext_vector_type(4))) float f32x4;
typedef __attribute__((ext_vector_type(4))) unsigned u32x4;
typedef __attribute__((ext_vector_type(2))) unsigned u32x2;
typedef __attribute__((ext_vector_type(2))) __bf16 bf2;
typedef unsigned short u16;

#define AS1 __attribute__((address_space(1)))
#define AS3 __attribute__((address_space(3)))

constexpr int S_ = 2048, D_ = 512, F_ = 1792;

__device__ __forceinline__ void glds16(unsigned voff, const void* sbase, unsigned lds) {
  unsigned keep;
  lds = __builtin_amdgcn_readfirstlane(lds);
  asm volatile("s_mov_b32 %0, m0\n\ts_mov_b32 m0, %3\n\ts_nop 0\n\tglobal_load_lds_dwordx4 %1, %2\n\ts_mov_b32 m0, %0"
               : "=&s"(keep) : "v"(voff), "s"(sbase), "s"(lds) : "memory");
}
__device__ __forceinline__ unsigned lds_addr(const char* p) { return (unsigned)(size_t)(const AS3 char*)p; }
#define WAIT_V(n) asm volatile("s_waitcnt vmcnt(%0)" ::"n"(n) : "memory")
#define WAIT_L0() asm volatile("s_waitcnt lgkmcnt(0)" ::: "memory")
#define BAR() asm volatile("s_barrier" ::: "memory")

__device__ __forceinline__ unsigned pack2(float a, float b) {
  typedef __attribute__((ext_vector_type(2))) float f32x2;
  f32x2 t = {a, b};
  return __builtin_bit_cast(unsigned, __builtin_convertvector(t, bf2));
}
__device__ __forceinline__ float rbf(float a) { return (float)(__bf16)a; }
__device__ __forceinline__ float lo16(unsigned u) { return __builtin_bit_cast(float, u << 16); }
__device__ __forceinline__ float hi16(unsigned u) { return __builtin_bit_cast(float, u & 0xffff0000u); }

__device__ __forceinline__ bf16x8 ld_b128(const char* p) { return *(const AS3 bf16x8*)(p); }
__device__ __forceinline__ bf16x8 ld_tr2(const char* p, int t_off) {
  s16x4 lo = __builtin_amdgcn_ds_read_tr16_b64_v4i16((AS3 s16x4*)(p));
  s16x4 hi = __builtin_amdgcn_ds_read_tr16_b64_v4i16((AS3 s16x4*)(p + t_off));
  return __builtin_shufflevector(lo, hi, 0, 1, 2, 3, 4, 5, 6, 7);
}
#define MFMA(a, b, c) __builtin_amdgcn_mfma_f32_16x16x32_bf16(a, b, c, 0, 0, 0)

__device__ __forceinline__ void pf_line(unsigned voff, const void* sbase, unsigned lds) {
  unsigned keep;
  lds = __builtin_amdgcn_readfirstlane(lds);
  asm volatile("s_mov_b32 %0, m0\n\ts_mov_b32 m0, %3\n\ts_nop 0\n\tglobal_load_lds_dword %1, %2\n\ts_mov_b32 m0, %0"
               : "=&s"(keep) : "v"(voff), "s"(sbase), "s"(lds) : "memory");
}
__device__ __forceinline__ void glds16_sc1(unsigned voff, const void* sbase, unsigned lds) {
  unsigned keep;
  lds = __builtin_amdgcn_readfirstlane(lds);
  asm volatile("s_mov_b32 %0, m0\n\ts_mov_b32 m0, %3\n\ts_nop 0\n\tglobal_load_lds_dwordx4 %1, %2 sc1\n\ts_mov_b32 m0, %0"
               : "=&s"(keep) : "v"(voff), "s"(sbase), "s"(lds) : "memory");
}
__device__ __forceinline__ void st16_sc1(void* p, u32x4 v) {
  asm volatile("global_store_dwordx4 %0, %1, off sc1\n\ts_nop 1" ::"v"(p), "v"(v) : "memory");
}
typedef AS1 unsigned gu32;
constexpr int FLAG_STRIDE = 32;

__device__ __forceinline__ unsigned src_off_ks(int ins, int lane, int ld) {
  int r = ins * 8 + (lane >> 3);
  int h = ((r >> 1) & 1) | (((r >> 3) & 1) << 1);
  int ch = (lane & 7) ^ (2 * h);
  return (unsigned)(r * ld + ch * 8);
}
__device__ __forceinline__ unsigned src_off_kc(int ins, int lane, int ld) {
  int r = ins * 8 + (lane >> 3);
  int ch = (lane & 7) ^ ((r >> 1) & 7);
  return (unsigned)(r * ld + ch * 8);
}
__device__ __forceinline__ int frag_off_ks(int lane, int ct) {
  int g = lane >> 4, q = (lane & 15) >> 2, p = lane & 3;
  int h = ((q >> 1) & 1) | ((g & 1) << 1);
  return (8 * g + q) * 128 + (((ct ^ h) * 2 + (p >> 1)) * 16) + 8 * (p & 1);
}
__device__ __forceinline__ int frag_off_kc(int lane, int ks) {
  int g = lane >> 4, l15 = lane & 15;
  return l15 * 128 + (((ks * 4 + g) ^ (l15 >> 1)) * 16);
}

template <int CTRL> __device__ __forceinline__ float dpp_f(float v) {
  return __builtin_bit_cast(float, __builtin_amdgcn_update_dpp(0, __builtin_bit_cast(int, v), CTRL, 0xf, 0xf, true));
}
__device__ __forceinline__ void sw16(float v, float& a, float& b) {
  unsigned u = __builtin_bit_cast(unsigned, v);
  auto r = __builtin_amdgcn_permlane16_swap(u, u, false, false);
  a = __builtin_bit_cast(float, (unsigned)r[0]); b = __builtin_bit_cast(float, (unsigned)r[1]);
}
__device__ __forceinline__ void sw32(float v, float& a, float& b) {
  unsigned u = __builtin_bit_cast(unsigned, v);
  auto r = __builtin_amdgcn_permlane32_swap(u, u, false, false);
  a = __builtin_bit_cast(float, (unsigned)r[0]); b = __builtin_bit_cast(float, (unsigned)r[1]);
}
__device__ __forceinline__ float max3(float a, float b, float c) {
  float r;
  asm("v_max3_f32 %0, %1, %2, %3" : "=v"(r) : "v"(a), "v"(b), "v"(c));
  return r;
}
__device__ __forceinline__ float sum32(float v) {
  v += dpp_f<0xB1>(v);
  v += dpp_f<0x4E>(v);
  v += dpp_f<0x141>(v);
  v += dpp_f<0x140>(v);
  float a, b;
  sw16(v, a, b);
  return a + b;
}
#define SB0() __builtin_amdgcn_sched_barrier(0)
#define USE_ILV 1
#define ILV0() SB0()
#define ILV(n, r) do { interleave<n, r, 8 - n>(); SB0(); } while (0)
template <int N, int R, int M> __device__ __forceinline__ void interleave() {
#pragma unroll
  for (int i = 0; i < N; ++i) { __builtin_amdgcn_sched_group_barrier(0x008, 1, 0); __builtin_amdgcn_sched_group_barrier(0x100, R, 0); }
  if (M > 0) __builtin_amdgcn_sched_group_barrier(0x008, M, 0);
}
__device__ __forceinline__ float gelu_tanh(float v) {
  const float c1 = -2.f * 0.7978845608028654f * 1.4426950408889634f, c2 = c1 * 0.044715f;
  float e = __builtin_amdgcn_exp2f(v * __builtin_fmaf(v * v, c2, c1));
  return v * __builtin_amdgcn_rcpf(1.f + e);
}

constexpr int K1_STAGE = 34816;
constexpr int K1_NST = 4;
#define WOPF 1
#define NI0 2
#define K1PF 5
#define MFMA16(a, b, c) __builtin_amdgcn_mfma_f32_16x16x16bf16_1k(a, b, c, 0, 0, 0)
__device__ __forceinline__ s16x4 pack4(f32x4 v) {
  u32x2 t = {pack2(v[0], v[1]), pack2(v[2], v[3])};
  return __builtin_bit_cast(s16x4, t);
}
__global__ __launch_bounds__(512, 2) void k1_qkv_attn_ln(const u16* __restrict__ x, const u16* __restrict__ wqkv,
                                                         const u16* __restrict__ gate_w, u16* __restrict__ o,
                                                         u16* __restrict__ hbuf, float* __restrict__ gbuf,
                                                         const u16* __restrict__ w1, const u16* __restrict__ wo,
                                                         unsigned* __restrict__ flags) {
  __shared__ __attribute__((aligned(16))) char smem[K1_STAGE * K1_NST + 256 + 4096];
  const int tid = threadIdx.x, lane = tid & 63;
  const int wv = __builtin_amdgcn_readfirstlane(tid >> 6);
  const int w = wv & 3;
  const int b = blockIdx.x, xcd = b & 7, idx = b >> 3;
  const int rt = (xcd >> 1) * 8 + (idx & 7), hg = (xcd & 1) * 4 + (idx >> 3);
  const int s0 = rt * 64;
  const int g = lane >> 4, l15 = lane & 15;
  const unsigned lbase = lds_addr(smem);
  const int rot = ((idx & 7) + 2 * (idx >> 3)) & 7;

  if (wv >= 4) {
    unsigned gw[8];
    unsigned gp[9];
#pragma unroll
    for (int m = 0; m < 9; ++m) {
      int j = w + 4 * m;
      if (j < 24) {
        gp[m] = ((j >> 3) * D_ + hg * 64 + src_off_ks(j & 7, lane, 3 * D_)) * 2u;
      } else {
        int jj = j - 24;
        jj = jj > 9 ? 9 : jj;
        int r = jj * 8 + (lane >> 3);
        int ch = (lane & 7) ^ ((r >> 1) & 7);
        int srow = s0 - 16 + r;
        srow = srow < 0 ? 0 : srow;
        gp[m] = (unsigned)(srow * D_ + ch * 8) * 2u;
      }
    }
    auto issue = [&](int st) {
      const unsigned dst = lbase + (st % K1_NST) * K1_STAGE + w * 1024;
      const int sr = (st + rot) & 7;
      const u16* xa = x + sr * 64;
      const u16* wa = wqkv + (size_t)sr * 64 * 3 * D_;
      glds16(gp[6], xa, dst + 6 * 4096);
      glds16(gp[7], xa, dst + 7 * 4096);
      if (w < 2) glds16(gp[8], xa, dst + 8 * 4096);
#pragma unroll
      for (int m = 0; m < 6; ++m) glds16(gp[m], wa, dst + m * 4096);
    };
    const int lnr = 16 + hg * 8 + 2 * w + (lane >> 5);
    const int lnoff = 24576 + lnr * 128 + ((((lane & 31) >> 2) ^ ((lnr >> 1) & 7)) * 16) + (lane & 3) * 4;
    unsigned xs[8];
    issue(0); issue(1);
    if (NI0 > 2) issue(2);
#pragma unroll
    for (int it = 0; it < 8; ++it) {
      constexpr int NI[9] = {NI0, 4, 5, 6, 7, 8, 8, 8, 8};
      const int ahead = NI[it] - it - 1;
      const int npf = it > K1PF ? 3 + WOPF : 0;
      WAIT_L0();
      if (w < 2) { WAIT_V(ahead * 9 + npf); } else { WAIT_V(ahead * 8 + npf); }
      BAR();
#pragma unroll
      for (int st = NI[it]; st < NI[it + 1]; ++st) issue(st);
      if (it == K1PF) {
        glds16(lane * 16u, gate_w, lbase + K1_STAGE * K1_NST + 256 + w * 1024);
        const unsigned dmy = lbase + K1_STAGE * K1_NST;
        const int ln = (lane & 7) < 7 ? (lane & 7) : 6;
        const unsigned ro = (unsigned)((idx * 64 + w * 16 + (lane >> 3)) * F_ + (xcd >> 1) * 448) * 2u + ln * 128;
#pragma unroll
        for (int i = 0; i < 2; ++i) pf_line(ro + i * 8 * F_ * 2, w1, dmy);
        pf_line((unsigned)(idx * 128 + w * 32 + (lane & 31)) * 128u, wo, dmy);
      }
      xs[it] = *(const AS3 unsigned*)(smem + (it % K1_NST) * K1_STAGE + lnoff);
    }
    {
      WAIT_V(2 + WOPF);
#pragma unroll
      for (int i = 0; i < 8; ++i) gw[i] = *(const AS3 unsigned*)(smem + K1_STAGE * K1_NST + 256 + w * 1024 + ((i + rot) & 7) * 128 + (lane & 31) * 4);
      float xv[16];
      float sum = 0.f, gs = 0.f;
#pragma unroll
      for (int i = 0; i < 8; ++i) {
        xv[2 * i] = lo16(xs[i]);
        xv[2 * i + 1] = hi16(xs[i]);
        sum += xv[2 * i] + xv[2 * i + 1];
        gs += xv[2 * i] * lo16(gw[i]) + xv[2 * i + 1] * hi16(gw[i]);
      }
      sum = sum32(sum);
      gs = sum32(gs);
      float mean = sum * (1.f / 512.f);
      float vs = 0.f;
#pragma unroll
      for (int i = 0; i < 16; ++i) { float d = xv[i] - mean; vs += d * d; }
      vs = sum32(vs);
      float rden = 1.f / sqrtf(vs * (1.f / 512.f) + 1e-5f);
      const int srow = s0 + hg * 8 + 2 * w + (lane >> 5);
#pragma unroll
      for (int i = 0; i < 8; ++i) {
        float a0 = (xv[2 * i] - mean) * rden, a1 = (xv[2 * i + 1] - mean) * rden;
        *(unsigned*)(hbuf + (size_t)srow * D_ + ((i + rot) & 7) * 64 + (lane & 31) * 2) = pack2(a0, a1);
      }
      if ((lane & 31) == 0) gbuf[srow] = 1.f / (1.f + __expf(-gs));
    }
  } else {
    const int offw = frag_off_ks(lane, w);
    int xoff[2];
#pragma unroll
    for (int ks = 0; ks < 2; ++ks) xoff[ks] = 24576 + frag_off_kc(lane, ks);
    f32x4 aq[4], ak[5], av[5];
#pragma unroll
    for (int i = 0; i < 5; ++i) { ak[i] = f32x4{0.f, 0.f, 0.f, 0.f}; av[i] = f32x4{0.f, 0.f, 0.f, 0.f}; if (i < 4) aq[i] = f32x4{0.f, 0.f, 0.f, 0.f}; }
    bf16x8 fw[2][3], fx[2][5];
    auto rd = [&](int it, int ks) {
      const char* sb = smem + (it % K1_NST) * K1_STAGE;
#pragma unroll
      for (int m = 0; m < 3; ++m) fw[ks][m] = ld_tr2(sb + m * 8192 + offw + ks * 4096, 512);
#pragma unroll
      for (int r = 0; r < 5; ++r) fx[ks][r] = ld_b128(sb + xoff[ks] + r * 2048);
    };
    auto mm = [&](int ks) {
#pragma unroll
      for (int r = 0; r < 5; ++r) {
        if (r > 0) aq[r - 1] = MFMA(fw[ks][0], fx[ks][r], aq[r - 1]);
        ak[r] = MFMA(fw[ks][1], fx[ks][r], ak[r]);
        av[r] = MFMA(fx[ks][r], fw[ks][2], av[r]);
      }
    };
    BAR();
    if (b == 0) flags[tid] = 0u;
    rd(0, 0);
#pragma unroll 1
    for (int it = 0; it < 7; ++it) {
      SB0();
      rd(it, 1);
      mm(0);
      interleave<6, 2, 8>();
      SB0();
      WAIT_L0();
      BAR();
      SB0();
      rd(it + 1, 0);
      mm(1);
      interleave<6, 2, 8>();
    }
    SB0();
    rd(7, 1);
    mm(0);
    interleave<6, 2, 8>();
    SB0();
    mm(1);
    s16x4 kb[5], vb[5];
#pragma unroll
    for (int r = 0; r < 5; ++r) { kb[r] = pack4(ak[r]); vb[r] = pack4(av[r]); }
#pragma unroll
    for (int t = 0; t < 4; ++t) {
      s16x4 qb = pack4(aq[t]);
      f32x4 z = {0.f, 0.f, 0.f, 0.f};
      f32x4 sp = MFMA16(kb[t], qb, z);
      f32x4 sc = MFMA16(kb[t + 1], qb, z);
      float lg[8];
      float mx = -3.0e38f;
#pragma unroll
      for (int j = 0; j < 4; ++j) {
        const int dp = l15 + 16 - (4 * g + j), dc = l15 - (4 * g + j);
        const bool vp = (dp <= 14) && !(dp & 1) && (s0 + t * 16 + l15 - dp >= 0);
        const bool vc = (dc >= 0) && (dc <= 14) && !(dc & 1);
        lg[j] = vp ? sp[j] : -3.0e38f;
        lg[4 + j] = vc ? sc[j] : -3.0e38f;
      }
      mx = max3(max3(max3(lg[0], lg[1], lg[2]), lg[3], lg[4]), max3(lg[5], lg[6], lg[7]), mx);
      { float a, c; sw16(mx, a, c); mx = max3(a, c, c); sw32(mx, a, c); mx = max3(a, c, c); }
      float sum = 0.f;
      const float cs = 0.25f * 1.4426950408889634f, mc = -mx * cs;
#pragma unroll
      for (int j = 0; j < 8; ++j) { lg[j] = __builtin_amdgcn_exp2f(__builtin_fmaf(lg[j], cs, mc)); sum += lg[j]; }
      { float a, c; sw16(sum, a, c); sum = a + c; sw32(sum, a, c); sum = a + c; }
      const float rs = __builtin_amdgcn_rcpf(sum);
      s16x4 pp = pack4(f32x4{lg[0] * rs, lg[1] * rs, lg[2] * rs, lg[3] * rs});
      s16x4 pc = pack4(f32x4{lg[4] * rs, lg[5] * rs, lg[6] * rs, lg[7] * rs});
      f32x4 ot = MFMA16(vb[t], pp, z);
      ot = MFMA16(vb[t + 1], pc, ot);
      u32x2 v = {pack2(ot[0], ot[1]), pack2(ot[2], ot[3])};
      *(u32x2*)(o + (size_t)(s0 + t * 16 + l15) * D_ + (hg * 4 + w) * 16 + 4 * g) = v;
    }
  }
}

constexpr int NST = 9;
#define EARLY 1
#define GLATE 0
#define SECT 0
#define OPF 1
#define A0A 3
#define A0C 3
#define EPOLL 1
#define PSLP 1
#define PUB_IT 0
#define ROTD 0

__device__ constexpr int n_issued(int j, int NK, int A0) {
  int a = A0 + 2 + 2 * j, c = (j - 1 + NST) > NST ? (j - 1 + NST) : NST;
  int m = a < c ? a : c;
  return m < NK ? m : NK;
}
__device__ constexpr int allowed(int done, int n, bool split) {
  int c = 0;
  for (int s = done + 1; s < n; ++s) c += (split && s < NST) ? 2 : 4;
  return c;
}

template <int NK, bool SPLIT, int A0, class ISSUE>
__device__ __forceinline__ void loader_stream(ISSUE issue) {
#pragma unroll
  for (int st = 0; st < n_issued(-1, NK, A0); ++st) issue(st, st, SPLIT);
  WAIT_V(allowed(0, n_issued(-1, NK, A0), SPLIT));
  BAR();
  constexpr int UN = (NST - 2) < (NK - 1) ? (NST - 2) : (NK - 1);
#pragma unroll
  for (int j = 0; j < UN; ++j) {
#pragma unroll
    for (int st = n_issued(j - 1, NK, A0); st < n_issued(j, NK, A0); ++st) issue(st, st % NST, SPLIT && st < NST);
    WAIT_V(allowed(j + 1, n_issued(j, NK, A0), SPLIT));
    BAR();
  }
  if constexpr (NK > NST) {
    int buf = (UN - 2) % NST;
#pragma unroll 1
    for (int it = UN; it < NK - NST + 2; ++it) {
      issue(it - 2 + NST, buf, false);
      buf = buf + 1 == NST ? 0 : buf + 1;
      WAIT_V((NST - 3) * 4);
      BAR();
    }
#pragma unroll
    for (int t = 0; t < NST - 3; ++t) {
      WAIT_V((NST - 4 - t) * 4);
      BAR();
    }
  }
}

__global__ __launch_bounds__(512, 2) void k2_mlp(const u16* __restrict__ hbuf, const u16* __restrict__ w1,
                                                 const u16* __restrict__ b1, u16* ubuf,
                                                 const u16* __restrict__ o, const u16* __restrict__ wo,
                                                 const u16* __restrict__ w2,
                                                 const u16* __restrict__ b2, const float* __restrict__ gbuf,
                                                 u16* __restrict__ out, unsigned* flags) {
  __shared__ __attribute__((aligned(16))) char smem[NST * 16384 + 8192 + 512];
  const int tid = threadIdx.x, lane = tid & 63;
  const int wv = __builtin_amdgcn_readfirstlane(tid >> 6);
  const int w = wv & 3;
  const int b = blockIdx.x, xcd = b & 7, idx = b >> 3;
  const int g = lane >> 4, l15 = lane & 15;
  const unsigned lbase = lds_addr(smem);
  const int dt = (xcd & 1) * 4 + (idx & 3), ft = (xcd >> 1) * 7 + (idx >> 2);
  const int stl = xcd * 4 + (idx & 3), dt4 = idx >> 2;
  const int s0 = stl * 64, d0 = dt4 * 64;
  const int wr = w >> 1, wc = w & 1;
  AS3 unsigned* sync_words = (AS3 unsigned*)(smem + NST * 16384 + 8192 + 256);

  if (wv >= 4) {
    unsigned pw1[2], ph[2], pwo[2], po[2], pu[2], pw2[2];
#pragma unroll
    for (int i = 0; i < 2; ++i) {
      pw1[i] = (ft * 64 + src_off_ks(2 * w + i, lane, F_)) * 2u;
      ph[i] = (dt * 64 + src_off_ks(2 * w + i, lane, D_)) * 2u;
      pwo[i] = (d0 + src_off_ks(2 * w + i, lane, D_)) * 2u;
      po[i] = (s0 * D_ + src_off_kc(2 * w + i, lane, D_)) * 2u;
      pu[i] = (d0 * F_ + src_off_kc(2 * w + i, lane, F_)) * 2u;
      pw2[i] = (s0 + src_off_ks(2 * w + i, lane, S_)) * 2u;
    }
    const int rotA = (((idx >> 2) * 32) / 7 + ROTD * (idx & 3)) & 31;
    auto issueB = [&](int st, int buf) {
      const unsigned dst = lbase + buf * 16384 + w * 2048;
      const int sr = (st + dt4) & 7;
      const u16* a0 = wo + (size_t)sr * 64 * D_;
      const u16* a1 = o + sr * 64;
      glds16(pwo[0], a0, dst);
      glds16(pwo[1], a0, dst + 1024);
      glds16(po[0], a1, dst + 8192);
      glds16(po[1], a1, dst + 8192 + 1024);
    };
    if (b < 224) {
      loader_stream<40, false, A0A>([&](int st, int buf, bool) {
        if (st < 32) {
          const unsigned dst = lbase + buf * 16384 + w * 2048;
          const int sr = (st + rotA) & 31;
          const u16* a0 = w1 + (size_t)sr * 64 * F_;
          const u16* a1 = hbuf + (size_t)sr * 64 * D_;
          glds16(pw1[0], a0, dst);
          glds16(pw1[1], a0, dst + 1024);
          glds16(ph[0], a1, dst + 8192);
          glds16(ph[1], a1, dst + 8192 + 1024);
        } else {
          issueB(st - 32, buf);
        }
      });
    } else {
      loader_stream<8, false, 3>([&](int st, int buf, bool) { issueB(st, buf); });
    }
    BAR();
#pragma unroll
    for (int st = 0; st < (EARLY ? NST : 0); ++st) {
      const unsigned dst = lbase + st * 16384 + w * 2048;
      const int sr = st;
      const u16* a0 = w2 + (size_t)sr * 64 * S_;
      glds16(pw2[0], a0, dst);
      glds16(pw2[1], a0, dst + 1024);
    }
    BAR();
    loader_stream<F_ / 64, EARLY, A0C>([&](int st, int buf, bool only1) {
      const unsigned dst = lbase + buf * 16384 + w * 2048;
      const int sr = st;
      const u16* a0 = w2 + (size_t)sr * 64 * S_;
      const u16* a1 = ubuf + sr * 64;
      if (!only1) {
        glds16(pw2[0], a0, dst);
        glds16(pw2[1], a0, dst + 1024);
      }
      glds16_sc1(pu[0], a1, dst + 8192);
      glds16_sc1(pu[1], a1, dst + 8192 + 1024);
    });
  } else {
    if (tid < 2) sync_words[tid] = 0u;
    int off_ks_r[2], off_ks_c[2], off_kc[2];
#pragma unroll
    for (int i = 0; i < 2; ++i) { off_ks_r[i] = frag_off_ks(lane, wr * 2 + i); off_ks_c[i] = frag_off_ks(lane, wc * 2 + i); off_kc[i] = frag_off_kc(lane, i); }
    f32x4 acc[2][2];
    bf16x8 fa[2][2][2], fb[2][2][2];
    auto zero_acc = [&]() {
#pragma unroll
      for (int i = 0; i < 2; ++i)
#pragma unroll
        for (int j = 0; j < 2; ++j) acc[i][j] = f32x4{0.f, 0.f, 0.f, 0.f};
    };
    auto mm = [&](int set) {
#pragma unroll
      for (int ks = 0; ks < 2; ++ks)
#pragma unroll
        for (int i = 0; i < 2; ++i)
#pragma unroll
          for (int j = 0; j < 2; ++j) acc[i][j] = MFMA(fa[set][ks][i], fb[set][ks][j], acc[i][j]);
    };
    int buf = 0;
    auto nextbuf = [&]() { buf = buf + 1 == NST ? 0 : buf + 1; };
    if (b < 224) {
      const int dd0 = dt * 64, f0 = ft * 64;
      constexpr int NK = S_ / 64;
      u32x2 bb[2];
      {
        const unsigned* bp = (const unsigned*)(b1 + f0 + wr * 32);
        unsigned t[16];
#pragma unroll
        for (int i = 0; i < 16; ++i) t[i] = bp[i];
#pragma unroll
        for (int i = 0; i < 2; ++i)
#pragma unroll
          for (int k = 0; k < 2; ++k) {
            unsigned lo = (g & 1) ? t[i * 8 + 2 + k] : t[i * 8 + k];
            unsigned hi = (g & 1) ? t[i * 8 + 6 + k] : t[i * 8 + 4 + k];
            bb[i][k] = (g & 2) ? hi : lo;
          }
      }
      zero_acc();
      auto rd = [&](int set) {
        const char* sb = smem + buf * 16384;
#pragma unroll
        for (int ks = 0; ks < 2; ++ks)
#pragma unroll
          for (int i = 0; i < 2; ++i) { fa[set][ks][i] = ld_tr2(sb + off_ks_r[i] + ks * 4096, 512); fb[set][ks][i] = ld_tr2(sb + 8192 + off_ks_c[i] + ks * 4096, 512); }
        nextbuf();
      };
      BAR();
      rd(0);
#pragma unroll 1
      for (int it = 0; it < NK - 2; it += 2) {
        BAR();
        ILV0();
        rd(1);
        mm(0);
        ILV(8, 2);
        BAR();
        ILV0();
        rd(0);
        mm(1);
        ILV(8, 2);
      }
      BAR();
      rd(1);
      mm(0);
      mm(1);
      {
        float bv[2][4];
#pragma unroll
        for (int i = 0; i < 2; ++i) { bv[i][0] = lo16(bb[i][0]); bv[i][1] = hi16(bb[i][0]); bv[i][2] = lo16(bb[i][1]); bv[i][3] = hi16(bb[i][1]); }
#pragma unroll
        for (int j = 0; j < 2; ++j) {
          unsigned p[2][2];
#pragma unroll
          for (int i = 0; i < 2; ++i) {
            float r[4];
#pragma unroll
            for (int e = 0; e < 4; ++e) r[e] = gelu_tanh(acc[i][j][e] + bv[i][e]);
            p[i][0] = pack2(r[0], r[1]); p[i][1] = pack2(r[2], r[3]);
          }
          auto q0 = __builtin_amdgcn_permlane16_swap(p[0][0], p[1][0], false, false);
          auto q1 = __builtin_amdgcn_permlane16_swap(p[0][1], p[1][1], false, false);
          u32x4 v = {(unsigned)q0[0], (unsigned)q1[0], (unsigned)q0[1], (unsigned)q1[1]};
          const int row = wc * 32 + j * 16 + l15;
          st16_sc1(ubuf + (size_t)(dd0 + row) * F_ + f0 + wr * 32 + (g & 1) * 16 + (g >> 1) * 8, v);
        }
      }
      if (PUB_IT < 0) {
        WAIT_V(0);
        if (lane == 0) {
          unsigned old = __hip_atomic_fetch_add(sync_words + 1, 1u, __ATOMIC_RELAXED, __HIP_MEMORY_SCOPE_WORKGROUP);
          if (old == 3u) __hip_atomic_store((gu32*)(flags + dt * FLAG_STRIDE + ft), 1u, __ATOMIC_RELAXED, __HIP_MEMORY_SCOPE_AGENT);
        }
      }
    } else {
      const unsigned dmy = lbase + NST * 16384 + 8192;
      const int wi = (idx - 28) * 4 + w;
      if (wi == 0) pf_line((unsigned)(xcd * 1024 + (lane & 7) * 128), gbuf, dmy);
      if (wi == 1) pf_line((unsigned)(xcd * 512 + (lane & 3) * 128), b2, dmy);
#pragma unroll
      for (int i = 0; i < 7; ++i)
        pf_line((unsigned)((i * 256 + wi * 16 + (lane >> 2)) * S_ + xcd * 256 + (lane & 3) * 64) * 2u, w2, dmy);
#pragma unroll
      for (int i = 0; i < 2; ++i) pf_line((unsigned)(xcd * 256 * D_ * 2 + (wi * 128 + i * 64 + lane) * 128), o, dmy);
    }
    f32x4 acca[2][2];
    gu32* fu = (gu32*)(flags + dt4 * FLAG_STRIDE + (lane < 28 ? lane : 27));
    unsigned vu = 0u;
    {
      zero_acc();
      auto rd = [&](int set) {
        const char* sb = smem + buf * 16384;
#pragma unroll
        for (int ks = 0; ks < 2; ++ks)
#pragma unroll
          for (int i = 0; i < 2; ++i) { fa[set][ks][i] = ld_tr2(sb + off_ks_r[i] + ks * 4096, 512); fb[set][ks][i] = ld_b128(sb + 8192 + wc * 4096 + off_kc[ks] + i * 2048); }
        nextbuf();
      };
      BAR();
      rd(0);
#pragma unroll 1
      for (int it = 0; it < 6; it += 2) {
        BAR();
        rd(1);
        mm(0);
        BAR();
        rd(0);
        if (it == PUB_IT && b < 224) {
          WAIT_V(0);
          if (lane == 0) {
            unsigned old = __hip_atomic_fetch_add(sync_words + 1, 1u, __ATOMIC_RELAXED, __HIP_MEMORY_SCOPE_WORKGROUP);
            if (old == 3u) __hip_atomic_store((gu32*)(flags + dt * FLAG_STRIDE + ft), 1u, __ATOMIC_RELAXED, __HIP_MEMORY_SCOPE_AGENT);
          }
        }
        mm(1);
      }
      if (wv == 0) vu = __hip_atomic_load(fu, __ATOMIC_RELAXED, __HIP_MEMORY_SCOPE_AGENT);
      BAR();
      rd(1);
      mm(0);
      mm(1);
#pragma unroll
      for (int i = 0; i < 2; ++i)
#pragma unroll
        for (int j = 0; j < 2; ++j) acca[i][j] = acc[i][j];
    }
    WAIT_L0();
    BAR();
    float gv[2], b2v[2];
#pragma unroll
    for (int j = 0; j < 2; ++j) {
      int s = s0 + wc * 32 + j * 16 + l15;
      gv[j] = gbuf[s];
      b2v[j] = lo16((unsigned)b2[s]);
    }
    if (wv == 0) {
      for (unsigned spin = 0; spin < (1u << 24); ++spin) {
        if (__builtin_amdgcn_ballot_w64(vu == 0u) == 0ull) break;
        __builtin_amdgcn_s_sleep(PSLP);
        vu = __hip_atomic_load(fu, __ATOMIC_RELAXED, __HIP_MEMORY_SCOPE_AGENT);
      }
    }
    BAR();
    {
      constexpr int NK = F_ / 64;
      zero_acc();
      buf = 0;
      auto rd = [&](int set) {
        const char* sb = smem + buf * 16384;
#pragma unroll
        for (int ks = 0; ks < 2; ++ks)
#pragma unroll
          for (int i = 0; i < 2; ++i) { fa[set][ks][i] = ld_b128(sb + 8192 + wr * 4096 + off_kc[ks] + i * 2048); fb[set][ks][i] = ld_tr2(sb + off_ks_c[i] + ks * 4096, 512); }
        nextbuf();
      };
      BAR();
      rd(0);
#pragma unroll 1
      for (int it = 0; it < NK - 2; it += 2) {
        BAR();
        ILV0();
        rd(1);
        mm(0);
        ILV(6, 2);
        BAR();
        ILV0();
        rd(0);
        mm(1);
        ILV(6, 2);
      }
      BAR();
      rd(1);
      mm(0);
      mm(1);
    }
#pragma unroll
    for (int j = 0; j < 2; ++j) {
      const int s = s0 + wc * 32 + j * 16 + l15;
      const float gg = gv[j], og = 1.f - gg;
      unsigned p[2][2];
#pragma unroll
      for (int i = 0; i < 2; ++i) {
        float r[4];
#pragma unroll
        for (int e = 0; e < 4; ++e) r[e] = gg * acca[i][j][e] + og * (acc[i][j][e] + b2v[j]);
        p[i][0] = pack2(r[0], r[1]); p[i][1] = pack2(r[2], r[3]);
      }
      auto q0 = __builtin_amdgcn_permlane16_swap(p[0][0], p[1][0], false, false);
      auto q1 = __builtin_amdgcn_permlane16_swap(p[0][1], p[1][1], false, false);
      u32x4 v = {(unsigned)q0[0], (unsigned)q1[0], (unsigned)q0[1], (unsigned)q1[1]};
      *(u32x4*)(out + (size_t)s * D_ + d0 + wr * 32 + (g & 1) * 16 + (g >> 1) * 8) = v;
    }
  }
}

extern "C" void kernel_launch(void* const* d_in, const int* in_sizes, int n_in, void* d_out, int out_size, void* d_ws,
                              size_t ws_size, hipStream_t stream) {
  const u16* x = (const u16*)d_in[0];
  const u16* wqkv = (const u16*)d_in[1];
  const u16* wo = (const u16*)d_in[2];
  const u16* w1 = (const u16*)d_in[4];
  const u16* b1 = (const u16*)d_in[5];
  const u16* w2 = (const u16*)d_in[6];
  const u16* b2 = (const u16*)d_in[7];
  const u16* gate_w = (const u16*)d_in[8];
  char* ws = (char*)d_ws;
  u16* obuf = (u16*)(ws);
  u16* hbuf = (u16*)(ws + (2 << 20));
  u16* ubuf = (u16*)(ws + (6 << 20));
  float* gbuf = (float*)(ws + (8 << 20));
  unsigned* flags = (unsigned*)(ws + (9 << 20));
  k1_qkv_attn_ln<<<256, 512, 0, stream>>>(x, wqkv, gate_w, obuf, hbuf, gbuf, w1, wo, flags);
  k2_mlp<<<256, 512, 0, stream>>>(hbuf, w1, b1, ubuf, obuf, wo, w2, b2, gbuf, (u16*)d_out, flags);
}
```

```cpp
#include <hip/hip_runtime.h>
#include <hip/hip_bf16.h>
#include <stdint.h>

typedef __attribute__((ext_vector_type(8))) short bf16x8;
typedef __attribute__((ext_vector_type(4))) short s16x4;
typedef __attribute__((ext_vector_type(4))) float f32x4;
typedef __attribute__((ext_vector_type(4))) unsigned u32x4;
typedef __attribute__((ext_vector_type(2))) unsigned u32x2;
typedef __attribute__((ext_vector_type(2))) __bf16 bf2;
typedef unsigned short u16;

#define AS1 __attribute__((address_space(1)))
#define AS3 __attribute__((address_space(3)))

constexpr int S_ = 2048, D_ = 512, F_ = 1792;

__device__ __forceinline__ void glds16(unsigned voff, const void* sbase, unsigned lds) {
  unsigned keep;
  lds = __builtin_amdgcn_readfirstlane(lds);
  asm volatile("s_mov_b32 %0, m0\n\ts_mov_b32 m0, %3\n\ts_nop 0\n\tglobal_load_lds_dwordx4 %1, %2\n\ts_mov_b32 m0, %0"
               : "=&s"(keep) : "v"(voff), "s"(sbase), "s"(lds) : "memory");
}
__device__ __forceinline__ unsigned lds_addr(const char* p) { return (unsigned)(size_t)(const AS3 char*)p; }
#define WAIT_V(n) asm volatile("s_waitcnt vmcnt(%0)" ::"n"(n) : "memory")
#define WAIT_L0() asm volatile("s_waitcnt lgkmcnt(0)" ::: "memory")
#define BAR() asm volatile("s_barrier" ::: "memory")

__device__ __forceinline__ unsigned pack2(float a, float b) {
  typedef __attribute__((ext_vector_type(2))) float f32x2;
  f32x2 t = {a, b};
  return __builtin_bit_cast(unsigned, __builtin_convertvector(t, bf2));
}
__device__ __forceinline__ float rbf(float a) { return (float)(__bf16)a; }
__device__ __forceinline__ float lo16(unsigned u) { return __builtin_bit_cast(float, u << 16); }
__device__ __forceinline__ float hi16(unsigned u) { return __builtin_bit_cast(float, u & 0xffff0000u); }

__device__ __forceinline__ bf16x8 ld_b128(const char* p) { return *(const AS3 bf16x8*)(p); }
__device__ __forceinline__ bf16x8 ld_tr2(const char* p, int t_off) {
  s16x4 lo = __builtin_amdgcn_ds_read_tr16_b64_v4i16((AS3 s16x4*)(p));
  s16x4 hi = __builtin_amdgcn_ds_read_tr16_b64_v4i16((AS3 s16x4*)(p + t_off));
  return __builtin_shufflevector(lo, hi, 0, 1, 2, 3, 4, 5, 6, 7);
}
#define MFMA(a, b, c) __builtin_amdgcn_mfma_f32_16x16x32_bf16(a, b, c, 0, 0, 0)

__device__ __forceinline__ void pf_line(unsigned voff, const void* sbase, unsigned lds) {
  unsigned keep;
  lds = __builtin_amdgcn_readfirstlane(lds);
  asm volatile("s_mov_b32 %0, m0\n\ts_mov_b32 m0, %3\n\ts_nop 0\n\tglobal_load_lds_dword %1, %2\n\ts_mov_b32 m0, %0"
               : "=&s"(keep) : "v"(voff), "s"(sbase), "s"(lds) : "memory");
}
__device__ __forceinline__ void glds16_sc1(unsigned voff, const void* sbase, unsigned lds) {
  unsigned keep;
  lds = __builtin_amdgcn_readfirstlane(lds);
  asm volatile("s_mov_b32 %0, m0\n\ts_mov_b32 m0, %3\n\ts_nop 0\n\tglobal_load_lds_dwordx4 %1, %2 sc1\n\ts_mov_b32 m0, %0"
               : "=&s"(keep) : "v"(voff), "s"(sbase), "s"(lds) : "memory");
}
__device__ __forceinline__ void st16_sc1(void* p, u32x4 v) {
  asm volatile("global_store_dwordx4 %0, %1, off sc1\n\ts_nop 1" ::"v"(p), "v"(v) : "memory");
}
typedef AS1 unsigned gu32;
constexpr int FLAG_STRIDE = 32;

__device__ __forceinline__ unsigned src_off_ks(int ins, int lane, int ld) {
  int r = ins * 8 + (lane >> 3);
  int h = ((r >> 1) & 1) | (((r >> 3) & 1) << 1);
  int ch = (lane & 7) ^ (2 * h);
  return (unsigned)(r * ld + ch * 8);
}
__device__ __forceinline__ unsigned src_off_kc(int ins, int lane, int ld) {
  int r = ins * 8 + (lane >> 3);
  int ch = (lane & 7) ^ ((r >> 1) & 7);
  return (unsigned)(r * ld + ch * 8);
}
__device__ __forceinline__ int frag_off_ks(int lane, int ct) {
  int g = lane >> 4, q = (lane & 15) >> 2, p = lane & 3;
  int h = ((q >> 1) & 1) | ((g & 1) << 1);
  return (8 * g + q) * 128 + (((ct ^ h) * 2 + (p >> 1)) * 16) + 8 * (p & 1);
}
__device__ __forceinline__ int frag_off_kc(int lane, int ks) {
  int g = lane >> 4, l15 = lane & 15;
  return l15 * 128 + (((ks * 4 + g) ^ (l15 >> 1)) * 16);
}

template <int CTRL> __device__ __forceinline__ float dpp_f(float v) {
  return __builtin_bit_cast(float, __builtin_amdgcn_update_dpp(0, __builtin_bit_cast(int, v), CTRL, 0xf, 0xf, true));
}
__device__ __forceinline__ void sw16(float v, float& a, float& b) {
  unsigned u = __builtin_bit_cast(unsigned, v);
  auto r = __builtin_amdgcn_permlane16_swap(u, u, false, false);
  a = __builtin_bit_cast(float, (unsigned)r[0]); b = __builtin_bit_cast(float, (unsigned)r[1]);
}
__device__ __forceinline__ void sw32(float v, float& a, float& b) {
  unsigned u = __builtin_bit_cast(unsigned, v);
  auto r = __builtin_amdgcn_permlane32_swap(u, u, false, false);
  a = __builtin_bit_cast(float, (unsigned)r[0]); b = __builtin_bit_cast(float, (unsigned)r[1]);
}
__device__ __forceinline__ float max3(float a, float b, float c) {
  float r;
  asm("v_max3_f32 %0, %1, %2, %3" : "=v"(r) : "v"(a), "v"(b), "v"(c));
  return r;
}
__device__ __forceinline__ float sum32(float v) {
  v += dpp_f<0xB1>(v);
  v += dpp_f<0x4E>(v);
  v += dpp_f<0x141>(v);
  v += dpp_f<0x140>(v);
  float a, b;
  sw16(v, a, b);
  return a + b;
}
#define SB0() __builtin_amdgcn_sched_barrier(0)
#define USE_ILV 1
#define ILV0() SB0()
#define ILV(n, r) do { interleave<n, r, 8 - n>(); SB0(); } while (0)
template <int N, int R, int M> __device__ __forceinline__ void interleave() {
#pragma unroll
  for (int i = 0; i < N; ++i) { __builtin_amdgcn_sched_group_barrier(0x008, 1, 0); __builtin_amdgcn_sched_group_barrier(0x100, R, 0); }
  if (M > 0) __builtin_amdgcn_sched_group_barrier(0x008, M, 0);
}
__device__ __forceinline__ float gelu_tanh(float v) {
  const float c1 = -2.f * 0.7978845608028654f * 1.4426950408889634f, c2 = c1 * 0.044715f;
  float e = __builtin_amdgcn_exp2f(v * __builtin_fmaf(v * v, c2, c1));
  return v * __builtin_amdgcn_rcpf(1.f + e);
}

constexpr int K1_STAGE = 34816;
constexpr int K1_NST = 4;
#define WOPF 1
#define TOUCH 0
#define K1PF 5
#define MFMA16(a, b, c) __builtin_amdgcn_mfma_f32_16x16x16bf16_1k(a, b, c, 0, 0, 0)
__device__ __forceinline__ s16x4 pack4(f32x4 v) {
  u32x2 t = {pack2(v[0], v[1]), pack2(v[2], v[3])};
  return __builtin_bit_cast(s16x4, t);
}
__global__ __launch_bounds__(512, 2) void k1_qkv_attn_ln(const u16* __restrict__ x, const u16* __restrict__ wqkv,
                                                         const u16* __restrict__ gate_w, u16* __restrict__ o,
                                                         u16* __restrict__ hbuf, float* __restrict__ gbuf,
                                                         const u16* __restrict__ w1, const u16* __restrict__ wo,
                                                         unsigned* __restrict__ flags, const u16* outp) {
  __shared__ __attribute__((aligned(16))) char smem[K1_STAGE * K1_NST + 256 + 4096];
  const int tid = threadIdx.x, lane = tid & 63;
  const int wv = __builtin_amdgcn_readfirstlane(tid >> 6);
  const int w = wv & 3;
  const int b = blockIdx.x, xcd = b & 7, idx = b >> 3;
  const int rt = (xcd >> 1) * 8 + (idx & 7), hg = (xcd & 1) * 4 + (idx >> 3);
  const int s0 = rt * 64;
  const int g = lane >> 4, l15 = lane & 15;
  const unsigned lbase = lds_addr(smem);
  const int rot = ((idx & 7) + 2 * (idx >> 3)) & 7;

  if (wv >= 4) {
    unsigned gw[8];
    unsigned gp[9];
#pragma unroll
    for (int m = 0; m < 9; ++m) {
      int j = w + 4 * m;
      if (j < 24) {
        gp[m] = ((j >> 3) * D_ + hg * 64 + src_off_ks(j & 7, lane, 3 * D_)) * 2u;
      } else {
        int jj = j - 24;
        jj = jj > 9 ? 9 : jj;
        int r = jj * 8 + (lane >> 3);
        int ch = (lane & 7) ^ ((r >> 1) & 7);
        int srow = s0 - 16 + r;
        srow = srow < 0 ? 0 : srow;
        gp[m] = (unsigned)(srow * D_ + ch * 8) * 2u;
      }
    }
    auto issue = [&](int st) {
      const unsigned dst = lbase + (st % K1_NST) * K1_STAGE + w * 1024;
      const int sr = (st + rot) & 7;
      const u16* xa = x + sr * 64;
      const u16* wa = wqkv + (size_t)sr * 64 * 3 * D_;
      glds16(gp[6], xa, dst + 6 * 4096);
      glds16(gp[7], xa, dst + 7 * 4096);
      if (w < 2) glds16(gp[8], xa, dst + 8 * 4096);
#pragma unroll
      for (int m = 0; m < 6; ++m) glds16(gp[m], wa, dst + m * 4096);
    };
    const int lnr = 16 + hg * 8 + 2 * w + (lane >> 5);
    const int lnoff = 24576 + lnr * 128 + ((((lane & 31) >> 2) ^ ((lnr >> 1) & 7)) * 16) + (lane & 3) * 4;
    unsigned xs[8];
    issue(0); issue(1);
    glds16(lane * 16u, gate_w, lbase + K1_STAGE * K1_NST + 256 + w * 1024);
#pragma unroll
    for (int it = 0; it < 8; ++it) {
      constexpr int NI[9] = {2, 4, 5, 6, 7, 8, 8, 8, 8};
      const int ahead = NI[it] - it - 1;
      const int npf = it > K1PF ? 2 + WOPF : it < 2 ? 1 + TOUCH : 0;
      WAIT_L0();
      if (w < 2) { WAIT_V(ahead * 9 + npf); } else { WAIT_V(ahead * 8 + npf); }
      BAR();
#pragma unroll
      for (int st = NI[it]; st < NI[it + 1]; ++st) issue(st);
      if (it == K1PF) {
        const unsigned dmy = lbase + K1_STAGE * K1_NST;
        const int ln = (lane & 7) < 7 ? (lane & 7) : 6;
        const unsigned ro = (unsigned)((idx * 64 + w * 16 + (lane >> 3)) * F_ + (xcd >> 1) * 448) * 2u + ln * 128;
#pragma unroll
        for (int i = 0; i < 2; ++i) pf_line(ro + i * 8 * F_ * 2, w1, dmy);
        pf_line((unsigned)(idx * 128 + w * 32 + (lane & 31)) * 128u, wo, dmy);
      }
      xs[it] = *(const AS3 unsigned*)(smem + (it % K1_NST) * K1_STAGE + lnoff);
    }
    {
#pragma unroll
      for (int i = 0; i < 8; ++i) gw[i] = *(const AS3 unsigned*)(smem + K1_STAGE * K1_NST + 256 + w * 1024 + ((i + rot) & 7) * 128 + (lane & 31) * 4);
      float xv[16];
      float sum = 0.f, gs = 0.f;
#pragma unroll
      for (int i = 0; i < 8; ++i) {
        xv[2 * i] = lo16(xs[i]);
        xv[2 * i + 1] = hi16(xs[i]);
        sum += xv[2 * i] + xv[2 * i + 1];
        gs += xv[2 * i] * lo16(gw[i]) + xv[2 * i + 1] * hi16(gw[i]);
      }
      sum = sum32(sum);
      gs = sum32(gs);
      float mean = sum * (1.f / 512.f);
      float vs = 0.f;
#pragma unroll
      for (int i = 0; i < 16; ++i) { float d = xv[i] - mean; vs += d * d; }
      vs = sum32(vs);
      float rden = 1.f / sqrtf(vs * (1.f / 512.f) + 1e-5f);
      const int srow = s0 + hg * 8 + 2 * w + (lane >> 5);
#pragma unroll
      for (int i = 0; i < 8; ++i) {
        float a0 = (xv[2 * i] - mean) * rden, a1 = (xv[2 * i + 1] - mean) * rden;
        *(unsigned*)(hbuf + (size_t)srow * D_ + ((i + rot) & 7) * 64 + (lane & 31) * 2) = pack2(a0, a1);
      }
      if ((lane & 31) == 0) gbuf[srow] = 1.f / (1.f + __expf(-gs));
    }
  } else {
    const int offw = frag_off_ks(lane, w);
    int xoff[2];
#pragma unroll
    for (int ks = 0; ks < 2; ++ks) xoff[ks] = 24576 + frag_off_kc(lane, ks);
    f32x4 aq[4], ak[5], av[5];
#pragma unroll
    for (int i = 0; i < 5; ++i) { ak[i] = f32x4{0.f, 0.f, 0.f, 0.f}; av[i] = f32x4{0.f, 0.f, 0.f, 0.f}; if (i < 4) aq[i] = f32x4{0.f, 0.f, 0.f, 0.f}; }
    bf16x8 fw[2][3], fx[2][5];
    auto rd = [&](int it, int ks) {
      const char* sb = smem + (it % K1_NST) * K1_STAGE;
#pragma unroll
      for (int m = 0; m < 3; ++m) fw[ks][m] = ld_tr2(sb + m * 8192 + offw + ks * 4096, 512);
#pragma unroll
      for (int r = 0; r < 5; ++r) fx[ks][r] = ld_b128(sb + xoff[ks] + r * 2048);
    };
    auto mm = [&](int ks) {
#pragma unroll
      for (int r = 0; r < 5; ++r) {
        if (r > 0) aq[r - 1] = MFMA(fw[ks][0], fx[ks][r], aq[r - 1]);
        ak[r] = MFMA(fw[ks][1], fx[ks][r], ak[r]);
        av[r] = MFMA(fx[ks][r], fw[ks][2], av[r]);
      }
    };
    BAR();
    if (b == 0) flags[tid] = 0u;
    rd(0, 0);
#pragma unroll 1
    for (int it = 0; it < 7; ++it) {
      SB0();
      rd(it, 1);
      mm(0);
      interleave<6, 2, 8>();
      SB0();
      WAIT_L0();
      BAR();
      SB0();
      rd(it + 1, 0);
      mm(1);
      interleave<6, 2, 8>();
    }
    SB0();
    rd(7, 1);
    mm(0);
    interleave<6, 2, 8>();
    SB0();
    mm(1);
    s16x4 kb[5], vb[5];
#pragma unroll
    for (int r = 0; r < 5; ++r) { kb[r] = pack4(ak[r]); vb[r] = pack4(av[r]); }
#pragma unroll
    for (int t = 0; t < 4; ++t) {
      s16x4 qb = pack4(aq[t]);
      f32x4 z = {0.f, 0.f, 0.f, 0.f};
      f32x4 sp = MFMA16(kb[t], qb, z);
      f32x4 sc = MFMA16(kb[t + 1], qb, z);
      float lg[8];
      float mx = -3.0e38f;
#pragma unroll
      for (int j = 0; j < 4; ++j) {
        const int dp = l15 + 16 - (4 * g + j), dc = l15 - (4 * g + j);
        const bool vp = (dp <= 14) && !(dp & 1) && (s0 + t * 16 + l15 - dp >= 0);
        const bool vc = (dc >= 0) && (dc <= 14) && !(dc & 1);
        lg[j] = vp ? sp[j] : -3.0e38f;
        lg[4 + j] = vc ? sc[j] : -3.0e38f;
      }
      mx = max3(max3(max3(lg[0], lg[1], lg[2]), lg[3], lg[4]), max3(lg[5], lg[6], lg[7]), mx);
      { float a, c; sw16(mx, a, c); mx = max3(a, c, c); sw32(mx, a, c); mx = max3(a, c, c); }
      float sum = 0.f;
      const float cs = 0.25f * 1.4426950408889634f, mc = -mx * cs;
#pragma unroll
      for (int j = 0; j < 8; ++j) { lg[j] = __builtin_amdgcn_exp2f(__builtin_fmaf(lg[j], cs, mc)); sum += lg[j]; }
      { float a, c; sw16(sum, a, c); sum = a + c; sw32(sum, a, c); sum = a + c; }
      const float rs = __builtin_amdgcn_rcpf(sum);
      s16x4 pp = pack4(f32x4{lg[0] * rs, lg[1] * rs, lg[2] * rs, lg[3] * rs});
      s16x4 pc = pack4(f32x4{lg[4] * rs, lg[5] * rs, lg[6] * rs, lg[7] * rs});
      f32x4 ot = MFMA16(vb[t], pp, z);
      ot = MFMA16(vb[t + 1], pc, ot);
      u32x2 v = {pack2(ot[0], ot[1]), pack2(ot[2], ot[3])};
      *(u32x2*)(o + (size_t)(s0 + t * 16 + l15) * D_ + (hg * 4 + w) * 16 + 4 * g) = v;
    }
  }
}

constexpr int NST = 9;
#define EARLY 1
#define GLATE 0
#define SECT 0
#define OPF 1
#define A0A 3
#define A0C 3
#define EPOLL 1
#define PSLP 1
#define PUB_IT 0
#define ROTD 0

__device__ constexpr int n_issued(int j, int NK, int A0) {
  int a = A0 + 2 + 2 * j, c = (j - 1 + NST) > NST ? (j - 1 + NST) : NST;
  int m = a < c ? a : c;
  return m < NK ? m : NK;
}
__device__ constexpr int allowed(int done, int n, bool split) {
  int c = 0;
  for (int s = done + 1; s < n; ++s) c += (split && s < NST) ? 2 : 4;
  return c;
}

template <int NK, bool SPLIT, int A0, class ISSUE>
__device__ __forceinline__ void loader_stream(ISSUE issue) {
#pragma unroll
  for (int st = 0; st < n_issued(-1, NK, A0); ++st) issue(st, st, SPLIT);
  WAIT_V(allowed(0, n_issued(-1, NK, A0), SPLIT));
  BAR();
  constexpr int UN = (NST - 2) < (NK - 1) ? (NST - 2) : (NK - 1);
#pragma unroll
  for (int j = 0; j < UN; ++j) {
#pragma unroll
    for (int st = n_issued(j - 1, NK, A0); st < n_issued(j, NK, A0); ++st) issue(st, st % NST, SPLIT && st < NST);
    WAIT_V(allowed(j + 1, n_issued(j, NK, A0), SPLIT));
    BAR();
  }
  if constexpr (NK > NST) {
    int buf = (UN - 2) % NST;
#pragma unroll 1
    for (int it = UN; it < NK - NST + 2; ++it) {
      issue(it - 2 + NST, buf, false);
      buf = buf + 1 == NST ? 0 : buf + 1;
      WAIT_V((NST - 3) * 4);
      BAR();
    }
#pragma unroll
    for (int t = 0; t < NST - 3; ++t) {
      WAIT_V((NST - 4 - t) * 4);
      BAR();
    }
  }
}

__global__ __launch_bounds__(512, 2) void k2_mlp(const u16* __restrict__ hbuf, const u16* __restrict__ w1,
                                                 const u16* __restrict__ b1, u16* ubuf,
                                                 const u16* __restrict__ o, const u16* __restrict__ wo,
                                                 const u16* __restrict__ w2,
                                                 const u16* __restrict__ b2, const float* __restrict__ gbuf,
                                                 u16* __restrict__ out, unsigned* flags) {
  __shared__ __attribute__((aligned(16))) char smem[NST * 16384 + 8192 + 512];
  const int tid = threadIdx.x, lane = tid & 63;
  const int wv = __builtin_amdgcn_readfirstlane(tid >> 6);
  const int w = wv & 3;
  const int b = blockIdx.x, xcd = b & 7, idx = b >> 3;
  const int g = lane >> 4, l15 = lane & 15;
  const unsigned lbase = lds_addr(smem);
  const int dt = (xcd & 1) * 4 + (idx & 3), ft = (xcd >> 1) * 7 + (idx >> 2);
  const int stl = xcd * 4 + (idx & 3), dt4 = idx >> 2;
  const int s0 = stl * 64, d0 = dt4 * 64;
  const int wr = w >> 1, wc = w & 1;
  AS3 unsigned* sync_words = (AS3 unsigned*)(smem + NST * 16384 + 8192 + 256);

  if (wv >= 4) {
    unsigned pw1[2], ph[2], pwo[2], po[2], pu[2], pw2[2];
#pragma unroll
    for (int i = 0; i < 2; ++i) {
      pw1[i] = (ft * 64 + src_off_ks(2 * w + i, lane, F_)) * 2u;
      ph[i] = (dt * 64 + src_off_ks(2 * w + i, lane, D_)) * 2u;
      pwo[i] = (d0 + src_off_ks(2 * w + i, lane, D_)) * 2u;
      po[i] = (s0 * D_ + src_off_kc(2 * w + i, lane, D_)) * 2u;
      pu[i] = (d0 * F_ + src_off_kc(2 * w + i, lane, F_)) * 2u;
      pw2[i] = (s0 + src_off_ks(2 * w + i, lane, S_)) * 2u;
    }
    const int rotA = (((idx >> 2) * 32) / 7 + ROTD * (idx & 3)) & 31;
    auto issueB = [&](int st, int buf) {
      const unsigned dst = lbase + buf * 16384 + w * 2048;
      const int sr = (st + dt4) & 7;
      const u16* a0 = wo + (size_t)sr * 64 * D_;
      const u16* a1 = o + sr * 64;
      glds16(pwo[0], a0, dst);
      glds16(pwo[1], a0, dst + 1024);
      glds16(po[0], a1, dst + 8192);
      glds16(po[1], a1, dst + 8192 + 1024);
    };
    if (b < 224) {
      loader_stream<40, false, A0A>([&](int st, int buf, bool) {
        if (st < 32) {
          const unsigned dst = lbase + buf * 16384 + w * 2048;
          const int sr = (st + rotA) & 31;
          const u16* a0 = w1 + (size_t)sr * 64 * F_;
          const u16* a1 = hbuf + (size_t)sr * 64 * D_;
          glds16(pw1[0], a0, dst);
          glds16(pw1[1], a0, dst + 1024);
          glds16(ph[0], a1, dst + 8192);
          glds16(ph[1], a1, dst + 8192 + 1024);
        } else {
          issueB(st - 32, buf);
        }
      });
    } else {
      loader_stream<8, false, 3>([&](int st, int buf, bool) { issueB(st, buf); });
    }
    BAR();
#pragma unroll
    for (int st = 0; st < (EARLY ? NST : 0); ++st) {
      const unsigned dst = lbase + st * 16384 + w * 2048;
      const int sr = st;
      const u16* a0 = w2 + (size_t)sr * 64 * S_;
      glds16(pw2[0], a0, dst);
      glds16(pw2[1], a0, dst + 1024);
    }
    BAR();
    loader_stream<F_ / 64, EARLY, A0C>([&](int st, int buf, bool only1) {
      const unsigned dst = lbase + buf * 16384 + w * 2048;
      const int sr = st;
      const u16* a0 = w2 + (size_t)sr * 64 * S_;
      const u16* a1 = ubuf + sr * 64;
      if (!only1) {
        glds16(pw2[0], a0, dst);
        glds16(pw2[1], a0, dst + 1024);
      }
      glds16_sc1(pu[0], a1, dst + 8192);
      glds16_sc1(pu[1], a1, dst + 8192 + 1024);
    });
  } else {
    if (tid < 2) sync_words[tid] = 0u;
    int off_ks_r[2], off_ks_c[2], off_kc[2];
#pragma unroll
    for (int i = 0; i < 2; ++i) { off_ks_r[i] = frag_off_ks(lane, wr * 2 + i); off_ks_c[i] = frag_off_ks(lane, wc * 2 + i); off_kc[i] = frag_off_kc(lane, i); }
    f32x4 acc[2][2];
    bf16x8 fa[2][2][2], fb[2][2][2];
    auto zero_acc = [&]() {
#pragma unroll
      for (int i = 0; i < 2; ++i)
#pragma unroll
        for (int j = 0; j < 2; ++j) acc[i][j] = f32x4{0.f, 0.f, 0.f, 0.f};
    };
    auto mm = [&](int set) {
#pragma unroll
      for (int ks = 0; ks < 2; ++ks)
#pragma unroll
        for (int i = 0; i < 2; ++i)
#pragma unroll
          for (int j = 0; j < 2; ++j) acc[i][j] = MFMA(fa[set][ks][i], fb[set][ks][j], acc[i][j]);
    };
    int buf = 0;
    auto nextbuf = [&]() { buf = buf + 1 == NST ? 0 : buf + 1; };
    if (b < 224) {
      const int dd0 = dt * 64, f0 = ft * 64;
      constexpr int NK = S_ / 64;
      u32x2 bb[2];
      {
        const unsigned* bp = (const unsigned*)(b1 + f0 + wr * 32);
        unsigned t[16];
#pragma unroll
        for (int i = 0; i < 16; ++i) t[i] = bp[i];
#pragma unroll
        for (int i = 0; i < 2; ++i)
#pragma unroll
          for (int k = 0; k < 2; ++k) {
            unsigned lo = (g & 1) ? t[i * 8 + 2 + k] : t[i * 8 + k];
            unsigned hi = (g & 1) ? t[i * 8 + 6 + k] : t[i * 8 + 4 + k];
            bb[i][k] = (g & 2) ? hi : lo;
          }
      }
      zero_acc();
      auto rd = [&](int set) {
        const char* sb = smem + buf * 16384;
#pragma unroll
        for (int ks = 0; ks < 2; ++ks)
#pragma unroll
          for (int i = 0; i < 2; ++i) { fa[set][ks][i] = ld_tr2(sb + off_ks_r[i] + ks * 4096, 512); fb[set][ks][i] = ld_tr2(sb + 8192 + off_ks_c[i] + ks * 4096, 512); }
        nextbuf();
      };
      BAR();
      rd(0);
#pragma unroll 1
      for (int it = 0; it < NK - 2; it += 2) {
        BAR();
        ILV0();
        rd(1);
        mm(0);
        ILV(8, 2);
        BAR();
        ILV0();
        rd(0);
        mm(1);
        ILV(8, 2);
      }
      BAR();
      rd(1);
      mm(0);
      mm(1);
      {
        float bv[2][4];
#pragma unroll
        for (int i = 0; i < 2; ++i) { bv[i][0] = lo16(bb[i][0]); bv[i][1] = hi16(bb[i][0]); bv[i][2] = lo16(bb[i][1]); bv[i][3] = hi16(bb[i][1]); }
#pragma unroll
        for (int j = 0; j < 2; ++j) {
          unsigned p[2][2];
#pragma unroll
          for (int i = 0; i < 2; ++i) {
            float r[4];
#pragma unroll
            for (int e = 0; e < 4; ++e) r[e] = gelu_tanh(acc[i][j][e] + bv[i][e]);
            p[i][0] = pack2(r[0], r[1]); p[i][1] = pack2(r[2], r[3]);
          }
          auto q0 = __builtin_amdgcn_permlane16_swap(p[0][0], p[1][0], false, false);
          auto q1 = __builtin_amdgcn_permlane16_swap(p[0][1], p[1][1], false, false);
          u32x4 v = {(unsigned)q0[0], (unsigned)q1[0], (unsigned)q0[1], (unsigned)q1[1]};
          const int row = wc * 32 + j * 16 + l15;
          st16_sc1(ubuf + (size_t)(dd0 + row) * F_ + f0 + wr * 32 + (g & 1) * 16 + (g >> 1) * 8, v);
        }
      }
      if (PUB_IT < 0) {
        WAIT_V(0);
        if (lane == 0) {
          unsigned old = __hip_atomic_fetch_add(sync_words + 1, 1u, __ATOMIC_RELAXED, __HIP_MEMORY_SCOPE_WORKGROUP);
          if (old == 3u) __hip_atomic_store((gu32*)(flags + dt * FLAG_STRIDE + ft), 1u, __ATOMIC_RELAXED, __HIP_MEMORY_SCOPE_AGENT);
        }
      }
    } else {
      const unsigned dmy = lbase + NST * 16384 + 8192;
      const int wi = (idx - 28) * 4 + w;
      if (wi == 0) pf_line((unsigned)(xcd * 1024 + (lane & 7) * 128), gbuf, dmy);
      if (wi == 1) pf_line((unsigned)(xcd * 512 + (lane & 3) * 128), b2, dmy);
#pragma unroll
      for (int i = 0; i < 7; ++i)
        pf_line((unsigned)((i * 256 + wi * 16 + (lane >> 2)) * S_ + xcd * 256 + (lane & 3) * 64) * 2u, w2, dmy);
#pragma unroll
      for (int i = 0; i < 2; ++i) pf_line((unsigned)(xcd * 256 * D_ * 2 + (wi * 128 + i * 64 + lane) * 128), o, dmy);
    }
    f32x4 acca[2][2];
    gu32* fu = (gu32*)(flags + dt4 * FLAG_STRIDE + (lane < 28 ? lane : 27));
    unsigned vu = 0u;
    {
      zero_acc();
      auto rd = [&](int set) {
        const char* sb = smem + buf * 16384;
#pragma unroll
        for (int ks = 0; ks < 2; ++ks)
#pragma unroll
          for (int i = 0; i < 2; ++i) { fa[set][ks][i] = ld_tr2(sb + off_ks_r[i] + ks * 4096, 512); fb[set][ks][i] = ld_b128(sb + 8192 + wc * 4096 + off_kc[ks] + i * 2048); }
        nextbuf();
      };
      BAR();
      rd(0);
#pragma unroll 1
      for (int it = 0; it < 6; it += 2) {
        BAR();
        rd(1);
        mm(0);
        BAR();
        rd(0);
        if (it == PUB_IT && b < 224) {
          WAIT_V(0);
          if (lane == 0) {
            unsigned old = __hip_atomic_fetch_add(sync_words + 1, 1u, __ATOMIC_RELAXED, __HIP_MEMORY_SCOPE_WORKGROUP);
            if (old == 3u) __hip_atomic_store((gu32*)(flags + dt * FLAG_STRIDE + ft), 1u, __ATOMIC_RELAXED, __HIP_MEMORY_SCOPE_AGENT);
          }
        }
        mm(1);
      }
      if (wv == 0) vu = __hip_atomic_load(fu, __ATOMIC_RELAXED, __HIP_MEMORY_SCOPE_AGENT);
      BAR();
      rd(1);
      mm(0);
      mm(1);
#pragma unroll
      for (int i = 0; i < 2; ++i)
#pragma unroll
        for (int j = 0; j < 2; ++j) acca[i][j] = acc[i][j];
    }
    WAIT_L0();
    BAR();
    float gv[2], b2v[2];
#pragma unroll
    for (int j = 0; j < 2; ++j) {
      int s = s0 + wc * 32 + j * 16 + l15;
      gv[j] = gbuf[s];
      b2v[j] = lo16((unsigned)b2[s]);
    }
    if (wv == 0) {
      for (unsigned spin = 0; spin < (1u << 24); ++spin) {
        if (__builtin_amdgcn_ballot_w64(vu == 0u) == 0ull) break;
        __builtin_amdgcn_s_sleep(PSLP);
        vu = __hip_atomic_load(fu, __ATOMIC_RELAXED, __HIP_MEMORY_SCOPE_AGENT);
      }
    }
    BAR();
    {
      constexpr int NK = F_ / 64;
      zero_acc();
      buf = 0;
      auto rd = [&](int set) {
        const char* sb = smem + buf * 16384;
#pragma unroll
        for (int ks = 0; ks < 2; ++ks)
#pragma unroll
          for (int i = 0; i < 2; ++i) { fa[set][ks][i] = ld_b128(sb + 8192 + wr * 4096 + off_kc[ks] + i * 2048); fb[set][ks][i] = ld_tr2(sb + off_ks_c[i] + ks * 4096, 512); }
        nextbuf();
      };
      BAR();
      rd(0);
#pragma unroll 1
      for (int it = 0; it < NK - 2; it += 2) {
        BAR();
        ILV0();
        rd(1);
        mm(0);
        ILV(6, 2);
        BAR();
        ILV0();
        rd(0);
        mm(1);
        ILV(6, 2);
      }
      BAR();
      rd(1);
      mm(0);
      mm(1);
    }
#pragma unroll
    for (int j = 0; j < 2; ++j) {
      const int s = s0 + wc * 32 + j * 16 + l15;
      const float gg = gv[j], og = 1.f - gg;
      unsigned p[2][2];
#pragma unroll
      for (int i = 0; i < 2; ++i) {
        float r[4];
#pragma unroll
        for (int e = 0; e < 4; ++e) r[e] = gg * acca[i][j][e] + og * (acc[i][j][e] + b2v[j]);
        p[i][0] = pack2(r[0], r[1]); p[i][1] = pack2(r[2], r[3]);
      }
      auto q0 = __builtin_amdgcn_permlane16_swap(p[0][0], p[1][0], false, false);
      auto q1 = __builtin_amdgcn_permlane16_swap(p[0][1], p[1][1], false, false);
      u32x4 v = {(unsigned)q0[0], (unsigned)q1[0], (unsigned)q0[1], (unsigned)q1[1]};
      *(u32x4*)(out + (size_t)s * D_ + d0 + wr * 32 + (g & 1) * 16 + (g >> 1) * 8) = v;
    }
  }
}

extern "C" void kernel_launch(void* const* d_in, const int* in_sizes, int n_in, void* d_out, int out_size, void* d_ws,
                              size_t ws_size, hipStream_t stream) {
  const u16* x = (const u16*)d_in[0];
  const u16* wqkv = (const u16*)d_in[1];
  const u16* wo = (const u16*)d_in[2];
  const u16* w1 = (const u16*)d_in[4];
  const u16* b1 = (const u16*)d_in[5];
  const u16* w2 = (const u16*)d_in[6];
  const u16* b2 = (const u16*)d_in[7];
  const u16* gate_w = (const u16*)d_in[8];
  char* ws = (char*)d_ws;
  u16* obuf = (u16*)(ws);
  u16* hbuf = (u16*)(ws + (2 << 20));
  u16* ubuf = (u16*)(ws + (6 << 20));
  float* gbuf = (float*)(ws + (8 << 20));
  unsigned* flags = (unsigned*)(ws + (9 << 20));
  k1_qkv_attn_ln<<<256, 512, 0, stream>>>(x, wqkv, gate_w, obuf, hbuf, gbuf, w1, wo, flags, (const u16*)d_out);
  k2_mlp<<<256, 512, 0, stream>>>(hbuf, w1, b1, ubuf, obuf, wo, w2, b2, gbuf, (u16*)d_out, flags);
}
```

```cpp
#include <hip/hip_runtime.h>
#include <hip/hip_bf16.h>
#include <stdint.h>

typedef __attribute__((ext_vector_type(8))) short bf16x8;
typedef __attribute__((ext_vector_type(4))) short s16x4;
typedef __attribute__((ext_vector_type(4))) float f32x4;
typedef __attribute__((ext_vector_type(4))) unsigned u32x4;
typedef __attribute__((ext_vector_type(2))) unsigned u32x2;
typedef __attribute__((ext_vector_type(2))) __bf16 bf2;
typedef unsigned short u16;

#define AS1 __attribute__((address_space(1)))
#define AS3 __attribute__((address_space(3)))

constexpr int S_ = 2048, D_ = 512, F_ = 1792;

__device__ __forceinline__ void glds16(unsigned voff, const void* sbase, unsigned lds) {
  unsigned keep;
  lds = __builtin_amdgcn_readfirstlane(lds);
  asm volatile("s_mov_b32 %0, m0\n\ts_mov_b32 m0, %3\n\ts_nop 0\n\tglobal_load_lds_dwordx4 %1, %2\n\ts_mov_b32 m0, %0"
               : "=&s"(keep) : "v"(voff), "s"(sbase), "s"(lds) : "memory");
}
__device__ __forceinline__ unsigned lds_addr(const char* p) { return (unsigned)(size_t)(const AS3 char*)p; }
#define WAIT_V(n) asm volatile("s_waitcnt vmcnt(%0)" ::"n"(n) : "memory")
#define WAIT_L0() asm volatile("s_waitcnt lgkmcnt(0)" ::: "memory")
#define BAR() asm volatile("s_barrier" ::: "memory")

__device__ __forceinline__ unsigned pack2(float a, float b) {
  typedef __attribute__((ext_vector_type(2))) float f32x2;
  f32x2 t = {a, b};
  return __builtin_bit_cast(unsigned, __builtin_convertvector(t, bf2));
}
__device__ __forceinline__ float rbf(float a) { return (float)(__bf16)a; }
__device__ __forceinline__ float lo16(unsigned u) { return __builtin_bit_cast(float, u << 16); }
__device__ __forceinline__ float hi16(unsigned u) { return __builtin_bit_cast(float, u & 0xffff0000u); }

__device__ __forceinline__ bf16x8 ld_b128(const char* p) { return *(const AS3 bf16x8*)(p); }
__device__ __forceinline__ bf16x8 ld_tr2(const char* p, int t_off) {
  s16x4 lo = __builtin_amdgcn_ds_read_tr16_b64_v4i16((AS3 s16x4*)(p));
  s16x4 hi = __builtin_amdgcn_ds_read_tr16_b64_v4i16((AS3 s16x4*)(p + t_off));
  return __builtin_shufflevector(lo, hi, 0, 1, 2, 3, 4, 5, 6, 7);
}
#define MFMA(a, b, c) __builtin_amdgcn_mfma_f32_16x16x32_bf16(a, b, c, 0, 0, 0)

__device__ __forceinline__ void pf_line(unsigned voff, const void* sbase, unsigned lds) {
  unsigned keep;
  lds = __builtin_amdgcn_readfirstlane(lds);
  asm volatile("s_mov_b32 %0, m0\n\ts_mov_b32 m0, %3\n\ts_nop 0\n\tglobal_load_lds_dword %1, %2\n\ts_mov_b32 m0, %0"
               : "=&s"(keep) : "v"(voff), "s"(sbase), "s"(lds) : "memory");
}
__device__ __forceinline__ void glds16_sc1(unsigned voff, const void* sbase, unsigned lds) {
  unsigned keep;
  lds = __builtin_amdgcn_readfirstlane(lds);
  asm volatile("s_mov_b32 %0, m0\n\ts_mov_b32 m0, %3\n\ts_nop 0\n\tglobal_load_lds_dwordx4 %1, %2 sc1\n\ts_mov_b32 m0, %0"
               : "=&s"(keep) : "v"(voff), "s"(sbase), "s"(lds) : "memory");
}
__device__ __forceinline__ void st16_sc1(void* p, u32x4 v) {
  asm volatile("global_store_dwordx4 %0, %1, off sc1\n\ts_nop 1" ::"v"(p), "v"(v) : "memory");
}
typedef AS1 unsigned gu32;
constexpr int FLAG_STRIDE = 32;

__device__ __forceinline__ unsigned src_off_ks(int ins, int lane, int ld) {
  int r = ins * 8 + (lane >> 3);
  int h = ((r >> 1) & 1) | (((r >> 3) & 1) << 1);
  int ch = (lane & 7) ^ (2 * h);
  return (unsigned)(r * ld + ch * 8);
}
__device__ __forceinline__ unsigned src_off_kc(int ins, int lane, int ld) {
  int r = ins * 8 + (lane >> 3);
  int ch = (lane & 7) ^ ((r >> 1) & 7);
  return (unsigned)(r * ld + ch * 8);
}
__device__ __forceinline__ int frag_off_ks(int lane, int ct) {
  int g = lane >> 4, q = (lane & 15) >> 2, p = lane & 3;
  int h = ((q >> 1) & 1) | ((g & 1) << 1);
  return (8 * g + q) * 128 + (((ct ^ h) * 2 + (p >> 1)) * 16) + 8 * (p & 1);
}
__device__ __forceinline__ int frag_off_kc(int lane, int ks) {
  int g = lane >> 4, l15 = lane & 15;
  return l15 * 128 + (((ks * 4 + g) ^ (l15 >> 1)) * 16);
}

template <int CTRL> __device__ __forceinline__ float dpp_f(float v) {
  return __builtin_bit_cast(float, __builtin_amdgcn_update_dpp(0, __builtin_bit_cast(int, v), CTRL, 0xf, 0xf, true));
}
__device__ __forceinline__ void sw16(float v, float& a, float& b) {
  unsigned u = __builtin_bit_cast(unsigned, v);
  auto r = __builtin_amdgcn_permlane16_swap(u, u, false, false);
  a = __builtin_bit_cast(float, (unsigned)r[0]); b = __builtin_bit_cast(float, (unsigned)r[1]);
}
__device__ __forceinline__ void sw32(float v, float& a, float& b) {
  unsigned u = __builtin_bit_cast(unsigned, v);
  auto r = __builtin_amdgcn_permlane32_swap(u, u, false, false);
  a = __builtin_bit_cast(float, (unsigned)r[0]); b = __builtin_bit_cast(float, (unsigned)r[1]);
}
__device__ __forceinline__ float max3(float a, float b, float c) {
  float r;
  asm("v_max3_f32 %0, %1, %2, %3" : "=v"(r) : "v"(a), "v"(b), "v"(c));
  return r;
}
__device__ __forceinline__ float sum32(float v) {
  v += dpp_f<0xB1>(v);
  v += dpp_f<0x4E>(v);
  v += dpp_f<0x141>(v);
  v += dpp_f<0x140>(v);
  float a, b;
  sw16(v, a, b);
  return a + b;
}
#define SB0() __builtin_amdgcn_sched_barrier(0)
#define USE_ILV 1
#define ILV0() SB0()
#define ILV(n, r) do { interleave<n, r, 8 - n>(); SB0(); } while (0)
template <int N, int R, int M> __device__ __forceinline__ void interleave() {
#pragma unroll
  for (int i = 0; i < N; ++i) { __builtin_amdgcn_sched_group_barrier(0x008, 1, 0); __builtin_amdgcn_sched_group_barrier(0x100, R, 0); }
  if (M > 0) __builtin_amdgcn_sched_group_barrier(0x008, M, 0);
}
__device__ __forceinline__ float gelu_tanh(float v) {
  const float c1 = -2.f * 0.7978845608028654f * 1.4426950408889634f, c2 = c1 * 0.044715f;
  float e = __builtin_amdgcn_exp2f(v * __builtin_fmaf(v * v, c2, c1));
  return v * __builtin_amdgcn_rcpf(1.f + e);
}

constexpr int K1_STAGE = 34816;
constexpr int K1_NST = 4;
#define WOPF 1
#define CODEPF 1
#define CODEPF1 0
#define K1PF 5
#define MFMA16(a, b, c) __builtin_amdgcn_mfma_f32_16x16x16bf16_1k(a, b, c, 0, 0, 0)
__device__ __forceinline__ s16x4 pack4(f32x4 v) {
  u32x2 t = {pack2(v[0], v[1]), pack2(v[2], v[3])};
  return __builtin_bit_cast(s16x4, t);
}
extern "C" __global__ __launch_bounds__(512, 2) void k1_qkv_attn_ln(const u16* __restrict__ x, const u16* __restrict__ wqkv,
                                                         const u16* __restrict__ gate_w, u16* __restrict__ o,
                                                         u16* __restrict__ hbuf, float* __restrict__ gbuf,
                                                         const u16* __restrict__ w1, const u16* __restrict__ wo,
                                                         unsigned* __restrict__ flags) {
  __shared__ __attribute__((aligned(16))) char smem[K1_STAGE * K1_NST + 256 + 4096];
  const int tid = threadIdx.x, lane = tid & 63;
  const int wv = __builtin_amdgcn_readfirstlane(tid >> 6);
  const int w = wv & 3;
  const int b = blockIdx.x, xcd = b & 7, idx = b >> 3;
  const int rt = (xcd >> 1) * 8 + (idx & 7), hg = (xcd & 1) * 4 + (idx >> 3);
  const int s0 = rt * 64;
  const int g = lane >> 4, l15 = lane & 15;
  const unsigned lbase = lds_addr(smem);
  const int rot = ((idx & 7) + 2 * (idx >> 3)) & 7;

  if (wv >= 4) {
    unsigned gw[8];
    unsigned gp[9];
#pragma unroll
    for (int m = 0; m < 9; ++m) {
      int j = w + 4 * m;
      if (j < 24) {
        gp[m] = ((j >> 3) * D_ + hg * 64 + src_off_ks(j & 7, lane, 3 * D_)) * 2u;
      } else {
        int jj = j - 24;
        jj = jj > 9 ? 9 : jj;
        int r = jj * 8 + (lane >> 3);
        int ch = (lane & 7) ^ ((r >> 1) & 7);
        int srow = s0 - 16 + r;
        srow = srow < 0 ? 0 : srow;
        gp[m] = (unsigned)(srow * D_ + ch * 8) * 2u;
      }
    }
    auto issue = [&](int st) {
      const unsigned dst = lbase + (st % K1_NST) * K1_STAGE + w * 1024;
      const int sr = (st + rot) & 7;
      const u16* xa = x + sr * 64;
      const u16* wa = wqkv + (size_t)sr * 64 * 3 * D_;
      glds16(gp[6], xa, dst + 6 * 4096);
      glds16(gp[7], xa, dst + 7 * 4096);
      if (w < 2) glds16(gp[8], xa, dst + 8 * 4096);
#pragma unroll
      for (int m = 0; m < 6; ++m) glds16(gp[m], wa, dst + m * 4096);
    };
    const int lnr = 16 + hg * 8 + 2 * w + (lane >> 5);
    const int lnoff = 24576 + lnr * 128 + ((((lane & 31) >> 2) ^ ((lnr >> 1) & 7)) * 16) + (lane & 3) * 4;
    unsigned xs[8];
    issue(0); issue(1);
    glds16(lane * 16u, gate_w, lbase + K1_STAGE * K1_NST + 256 + w * 1024);
#pragma unroll
    for (int it = 0; it < 8; ++it) {
      constexpr int NI[9] = {2, 4, 5, 6, 7, 8, 8, 8, 8};
      const int ahead = NI[it] - it - 1;
      const int npf = it > K1PF ? 2 + WOPF + CODEPF : it < 2 ? 1 + CODEPF1 : 0;
      WAIT_L0();
      if (w < 2) { WAIT_V(ahead * 9 + npf); } else { WAIT_V(ahead * 8 + npf); }
      BAR();
#pragma unroll
      for (int st = NI[it]; st < NI[it + 1]; ++st) issue(st);
      if (it == K1PF) {
        const unsigned dmy = lbase + K1_STAGE * K1_NST;
        const int ln = (lane & 7) < 7 ? (lane & 7) : 6;
        const unsigned ro = (unsigned)((idx * 64 + w * 16 + (lane >> 3)) * F_ + (xcd >> 1) * 448) * 2u + ln * 128;
#pragma unroll
        for (int i = 0; i < 2; ++i) pf_line(ro + i * 8 * F_ * 2, w1, dmy);
        pf_line((unsigned)(idx * 128 + w * 32 + (lane & 31)) * 128u, wo, dmy);
        {
          unsigned long long pc;
          unsigned rel;
          asm volatile("s_getpc_b64 %0\n\ts_mov_b32 %1, k2_mlp@rel32@lo+4" : "=s"(pc), "=s"(rel));
          pf_line((unsigned)(w * 24 + (lane < 24 ? lane : 23)) * 128u, (const void*)(pc + (long long)(int)rel), dmy);
        }
      }
      xs[it] = *(const AS3 unsigned*)(smem + (it % K1_NST) * K1_STAGE + lnoff);
    }
    {
#pragma unroll
      for (int i = 0; i < 8; ++i) gw[i] = *(const AS3 unsigned*)(smem + K1_STAGE * K1_NST + 256 + w * 1024 + ((i + rot) & 7) * 128 + (lane & 31) * 4);
      float xv[16];
      float sum = 0.f, gs = 0.f;
#pragma unroll
      for (int i = 0; i < 8; ++i) {
        xv[2 * i] = lo16(xs[i]);
        xv[2 * i + 1] = hi16(xs[i]);
        sum += xv[2 * i] + xv[2 * i + 1];
        gs += xv[2 * i] * lo16(gw[i]) + xv[2 * i + 1] * hi16(gw[i]);
      }
      sum = sum32(sum);
      gs = sum32(gs);
      float mean = sum * (1.f / 512.f);
      float vs = 0.f;
#pragma unroll
      for (int i = 0; i < 16; ++i) { float d = xv[i] - mean; vs += d * d; }
      vs = sum32(vs);
      float rden = 1.f / sqrtf(vs * (1.f / 512.f) + 1e-5f);
      const int srow = s0 + hg * 8 + 2 * w + (lane >> 5);
#pragma unroll
      for (int i = 0; i < 8; ++i) {
        float a0 = (xv[2 * i] - mean) * rden, a1 = (xv[2 * i + 1] - mean) * rden;
        *(unsigned*)(hbuf + (size_t)srow * D_ + ((i + rot) & 7) * 64 + (lane & 31) * 2) = pack2(a0, a1);
      }
      if ((lane & 31) == 0) gbuf[srow] = 1.f / (1.f + __expf(-gs));
    }
  } else {
    const int offw = frag_off_ks(lane, w);
    int xoff[2];
#pragma unroll
    for (int ks = 0; ks < 2; ++ks) xoff[ks] = 24576 + frag_off_kc(lane, ks);
    f32x4 aq[4], ak[5], av[5];
#pragma unroll
    for (int i = 0; i < 5; ++i) { ak[i] = f32x4{0.f, 0.f, 0.f, 0.f}; av[i] = f32x4{0.f, 0.f, 0.f, 0.f}; if (i < 4) aq[i] = f32x4{0.f, 0.f, 0.f, 0.f}; }
    bf16x8 fw[2][3], fx[2][5];
    auto rd = [&](int it, int ks) {
      const char* sb = smem + (it % K1_NST) * K1_STAGE;
#pragma unroll
      for (int m = 0; m < 3; ++m) fw[ks][m] = ld_tr2(sb + m * 8192 + offw + ks * 4096, 512);
#pragma unroll
      for (int r = 0; r < 5; ++r) fx[ks][r] = ld_b128(sb + xoff[ks] + r * 2048);
    };
    auto mm = [&](int ks) {
#pragma unroll
      for (int r = 0; r < 5; ++r) {
        if (r > 0) aq[r - 1] = MFMA(fw[ks][0], fx[ks][r], aq[r - 1]);
        ak[r] = MFMA(fw[ks][1], fx[ks][r], ak[r]);
        av[r] = MFMA(fx[ks][r], fw[ks][2], av[r]);
      }
    };
    BAR();
    if (b == 0) flags[tid] = 0u;
    rd(0, 0);
#pragma unroll 1
    for (int it = 0; it < 7; ++it) {
      SB0();
      rd(it, 1);
      mm(0);
      interleave<6, 2, 8>();
      SB0();
      WAIT_L0();
      BAR();
      SB0();
      rd(it + 1, 0);
      mm(1);
      interleave<6, 2, 8>();
    }
    SB0();
    rd(7, 1);
    mm(0);
    interleave<6, 2, 8>();
    SB0();
    mm(1);
    s16x4 kb[5], vb[5];
#pragma unroll
    for (int r = 0; r < 5; ++r) { kb[r] = pack4(ak[r]); vb[r] = pack4(av[r]); }
#pragma unroll
    for (int t = 0; t < 4; ++t) {
      s16x4 qb = pack4(aq[t]);
      f32x4 z = {0.f, 0.f, 0.f, 0.f};
      f32x4 sp = MFMA16(kb[t], qb, z);
      f32x4 sc = MFMA16(kb[t + 1], qb, z);
      float lg[8];
      float mx = -3.0e38f;
#pragma unroll
      for (int j = 0; j < 4; ++j) {
        const int dp = l15 + 16 - (4 * g + j), dc = l15 - (4 * g + j);
        const bool vp = (dp <= 14) && !(dp & 1) && (s0 + t * 16 + l15 - dp >= 0);
        const bool vc = (dc >= 0) && (dc <= 14) && !(dc & 1);
        lg[j] = vp ? sp[j] : -3.0e38f;
        lg[4 + j] = vc ? sc[j] : -3.0e38f;
      }
      mx = max3(max3(max3(lg[0], lg[1], lg[2]), lg[3], lg[4]), max3(lg[5], lg[6], lg[7]), mx);
      { float a, c; sw16(mx, a, c); mx = max3(a, c, c); sw32(mx, a, c); mx = max3(a, c, c); }
      float sum = 0.f;
      const float cs = 0.25f * 1.4426950408889634f, mc = -mx * cs;
#pragma unroll
      for (int j = 0; j < 8; ++j) { lg[j] = __builtin_amdgcn_exp2f(__builtin_fmaf(lg[j], cs, mc)); sum += lg[j]; }
      { float a, c; sw16(sum, a, c); sum = a + c; sw32(sum, a, c); sum = a + c; }
      const float rs = __builtin_amdgcn_rcpf(sum);
      s16x4 pp = pack4(f32x4{lg[0] * rs, lg[1] * rs, lg[2] * rs, lg[3] * rs});
      s16x4 pc = pack4(f32x4{lg[4] * rs, lg[5] * rs, lg[6] * rs, lg[7] * rs});
      f32x4 ot = MFMA16(vb[t], pp, z);
      ot = MFMA16(vb[t + 1], pc, ot);
      u32x2 v = {pack2(ot[0], ot[1]), pack2(ot[2], ot[3])};
      *(u32x2*)(o + (size_t)(s0 + t * 16 + l15) * D_ + (hg * 4 + w) * 16 + 4 * g) = v;
    }
  }
}

constexpr int NST = 9;
#define EARLY 1
#define GLATE 0
#define SECT 0
#define OPF 1
#define A0A 3
#define A0C 3
#define EPOLL 1
#define PSLP 1
#define PUB_IT 0
#define ROTD 0

__device__ constexpr int n_issued(int j, int NK, int A0) {
  int a = A0 + 2 + 2 * j, c = (j - 1 + NST) > NST ? (j - 1 + NST) : NST;
  int m = a < c ? a : c;
  return m < NK ? m : NK;
}
__device__ constexpr int allowed(int done, int n, bool split) {
  int c = 0;
  for (int s = done + 1; s < n; ++s) c += (split && s < NST) ? 2 : 4;
  return c;
}

template <int NK, bool SPLIT, int A0, class ISSUE>
__device__ __forceinline__ void loader_stream(ISSUE issue) {
#pragma unroll
  for (int st = 0; st < n_issued(-1, NK, A0); ++st) issue(st, st, SPLIT);
  WAIT_V(allowed(0, n_issued(-1, NK, A0), SPLIT));
  BAR();
  constexpr int UN = (NST - 2) < (NK - 1) ? (NST - 2) : (NK - 1);
#pragma unroll
  for (int j = 0; j < UN; ++j) {
#pragma unroll
    for (int st = n_issued(j - 1, NK, A0); st < n_issued(j, NK, A0); ++st) issue(st, st % NST, SPLIT && st < NST);
    WAIT_V(allowed(j + 1, n_issued(j, NK, A0), SPLIT));
    BAR();
  }
  if constexpr (NK > NST) {
    int buf = (UN - 2) % NST;
#pragma unroll 1
    for (int it = UN; it < NK - NST + 2; ++it) {
      issue(it - 2 + NST, buf, false);
      buf = buf + 1 == NST ? 0 : buf + 1;
      WAIT_V((NST - 3) * 4);
      BAR();
    }
#pragma unroll
    for (int t = 0; t < NST - 3; ++t) {
      WAIT_V((NST - 4 - t) * 4);
      BAR();
    }
  }
}

extern "C" __global__ __launch_bounds__(512, 2) void k2_mlp(const u16* __restrict__ hbuf, const u16* __restrict__ w1,
                                                 const u16* __restrict__ b1, u16* ubuf,
                                                 const u16* __restrict__ o, const u16* __restrict__ wo,
                                                 const u16* __restrict__ w2,
                                                 const u16* __restrict__ b2, const float* __restrict__ gbuf,
                                                 u16* __restrict__ out, unsigned* flags) {
  __shared__ __attribute__((aligned(16))) char smem[NST * 16384 + 8192 + 512];
  const int tid = threadIdx.x, lane = tid & 63;
  const int wv = __builtin_amdgcn_readfirstlane(tid >> 6);
  const int w = wv & 3;
  const int b = blockIdx.x, xcd = b & 7, idx = b >> 3;
  const int g = lane >> 4, l15 = lane & 15;
  const unsigned lbase = lds_addr(smem);
  const int dt = (xcd & 1) * 4 + (idx & 3), ft = (xcd >> 1) * 7 + (idx >> 2);
  const int stl = xcd * 4 + (idx & 3), dt4 = idx >> 2;
  const int s0 = stl * 64, d0 = dt4 * 64;
  const int wr = w >> 1, wc = w & 1;
  AS3 unsigned* sync_words = (AS3 unsigned*)(smem + NST * 16384 + 8192 + 256);

  if (wv >= 4) {
    unsigned pw1[2], ph[2], pwo[2], po[2], pu[2], pw2[2];
#pragma unroll
    for (int i = 0; i < 2; ++i) {
      pw1[i] = (ft * 64 + src_off_ks(2 * w + i, lane, F_)) * 2u;
      ph[i] = (dt * 64 + src_off_ks(2 * w + i, lane, D_)) * 2u;
      pwo[i] = (d0 + src_off_ks(2 * w + i, lane, D_)) * 2u;
      po[i] = (s0 * D_ + src_off_kc(2 * w + i, lane, D_)) * 2u;
      pu[i] = (d0 * F_ + src_off_kc(2 * w + i, lane, F_)) * 2u;
      pw2[i] = (s0 + src_off_ks(2 * w + i, lane, S_)) * 2u;
    }
    const int rotA = (((idx >> 2) * 32) / 7 + ROTD * (idx & 3)) & 31;
    auto issueB = [&](int st, int buf) {
      const unsigned dst = lbase + buf * 16384 + w * 2048;
      const int sr = (st + dt4) & 7;
      const u16* a0 = wo + (size_t)sr * 64 * D_;
      const u16* a1 = o + sr * 64;
      glds16(pwo[0], a0, dst);
      glds16(pwo[1], a0, dst + 1024);
      glds16(po[0], a1, dst + 8192);
      glds16(po[1], a1, dst + 8192 + 1024);
    };
    if (b < 224) {
      loader_stream<40, false, A0A>([&](int st, int buf, bool) {
        if (st < 32) {
          const unsigned dst = lbase + buf * 16384 + w * 2048;
          const int sr = (st + rotA) & 31;
          const u16* a0 = w1 + (size_t)sr * 64 * F_;
          const u16* a1 = hbuf + (size_t)sr * 64 * D_;
          glds16(pw1[0], a0, dst);
          glds16(pw1[1], a0, dst + 1024);
          glds16(ph[0], a1, dst + 8192);
          glds16(ph[1], a1, dst + 8192 + 1024);
        } else {
          issueB(st - 32, buf);
        }
      });
    } else {
      loader_stream<8, false, 3>([&](int st, int buf, bool) { issueB(st, buf); });
    }
    BAR();
#pragma unroll
    for (int st = 0; st < (EARLY ? NST : 0); ++st) {
      const unsigned dst = lbase + st * 16384 + w * 2048;
      const int sr = st;
      const u16* a0 = w2 + (size_t)sr * 64 * S_;
      glds16(pw2[0], a0, dst);
      glds16(pw2[1], a0, dst + 1024);
    }
    BAR();
    loader_stream<F_ / 64, EARLY, A0C>([&](int st, int buf, bool only1) {
      const unsigned dst = lbase + buf * 16384 + w * 2048;
      const int sr = st;
      const u16* a0 = w2 + (size_t)sr * 64 * S_;
      const u16* a1 = ubuf + sr * 64;
      if (!only1) {
        glds16(pw2[0], a0, dst);
        glds16(pw2[1], a0, dst + 1024);
      }
      glds16_sc1(pu[0], a1, dst + 8192);
      glds16_sc1(pu[1], a1, dst + 8192 + 1024);
    });
  } else {
    if (tid < 2) sync_words[tid] = 0u;
    int off_ks_r[2], off_ks_c[2], off_kc[2];
#pragma unroll
    for (int i = 0; i < 2; ++i) { off_ks_r[i] = frag_off_ks(lane, wr * 2 + i); off_ks_c[i] = frag_off_ks(lane, wc * 2 + i); off_kc[i] = frag_off_kc(lane, i); }
    f32x4 acc[2][2];
    bf16x8 fa[2][2][2], fb[2][2][2];
    auto zero_acc = [&]() {
#pragma unroll
      for (int i = 0; i < 2; ++i)
#pragma unroll
        for (int j = 0; j < 2; ++j) acc[i][j] = f32x4{0.f, 0.f, 0.f, 0.f};
    };
    auto mm = [&](int set) {
#pragma unroll
      for (int ks = 0; ks < 2; ++ks)
#pragma unroll
        for (int i = 0; i < 2; ++i)
#pragma unroll
          for (int j = 0; j < 2; ++j) acc[i][j] = MFMA(fa[set][ks][i], fb[set][ks][j], acc[i][j]);
    };
    int buf = 0;
    auto nextbuf = [&]() { buf = buf + 1 == NST ? 0 : buf + 1; };
    if (b < 224) {
      const int dd0 = dt * 64, f0 = ft * 64;
      constexpr int NK = S_ / 64;
      u32x2 bb[2];
      {
        const unsigned* bp = (const unsigned*)(b1 + f0 + wr * 32);
        unsigned t[16];
#pragma unroll
        for (int i = 0; i < 16; ++i) t[i] = bp[i];
#pragma unroll
        for (int i = 0; i < 2; ++i)
#pragma unroll
          for (int k = 0; k < 2; ++k) {
            unsigned lo = (g & 1) ? t[i * 8 + 2 + k] : t[i * 8 + k];
            unsigned hi = (g & 1) ? t[i * 8 + 6 + k] : t[i * 8 + 4 + k];
            bb[i][k] = (g & 2) ? hi : lo;
          }
      }
      zero_acc();
      auto rd = [&](int set) {
        const char* sb = smem + buf * 16384;
#pragma unroll
        for (int ks = 0; ks < 2; ++ks)
#pragma unroll
          for (int i = 0; i < 2; ++i) { fa[set][ks][i] = ld_tr2(sb + off_ks_r[i] + ks * 4096, 512); fb[set][ks][i] = ld_tr2(sb + 8192 + off_ks_c[i] + ks * 4096, 512); }
        nextbuf();
      };
      BAR();
      rd(0);
#pragma unroll 1
      for (int it = 0; it < NK - 2; it += 2) {
        BAR();
        ILV0();
        rd(1);
        mm(0);
        ILV(8, 2);
        BAR();
        ILV0();
        rd(0);
        mm(1);
        ILV(8, 2);
      }
      BAR();
      rd(1);
      mm(0);
      mm(1);
      {
        float bv[2][4];
#pragma unroll
        for (int i = 0; i < 2; ++i) { bv[i][0] = lo16(bb[i][0]); bv[i][1] = hi16(bb[i][0]); bv[i][2] = lo16(bb[i][1]); bv[i][3] = hi16(bb[i][1]); }
#pragma unroll
        for (int j = 0; j < 2; ++j) {
          unsigned p[2][2];
#pragma unroll
          for (int i = 0; i < 2; ++i) {
            float r[4];
#pragma unroll
            for (int e = 0; e < 4; ++e) r[e] = gelu_tanh(acc[i][j][e] + bv[i][e]);
            p[i][0] = pack2(r[0], r[1]); p[i][1] = pack2(r[2], r[3]);
          }
          auto q0 = __builtin_amdgcn_permlane16_swap(p[0][0], p[1][0], false, false);
          auto q1 = __builtin_amdgcn_permlane16_swap(p[0][1], p[1][1], false, false);
          u32x4 v = {(unsigned)q0[0], (unsigned)q1[0], (unsigned)q0[1], (unsigned)q1[1]};
          const int row = wc * 32 + j * 16 + l15;
          st16_sc1(ubuf + (size_t)(dd0 + row) * F_ + f0 + wr * 32 + (g & 1) * 16 + (g >> 1) * 8, v);
        }
      }
      if (PUB_IT < 0) {
        WAIT_V(0);
        if (lane == 0) {
          unsigned old = __hip_atomic_fetch_add(sync_words + 1, 1u, __ATOMIC_RELAXED, __HIP_MEMORY_SCOPE_WORKGROUP);
          if (old == 3u) __hip_atomic_store((gu32*)(flags + dt * FLAG_STRIDE + ft), 1u, __ATOMIC_RELAXED, __HIP_MEMORY_SCOPE_AGENT);
        }
      }
    } else {
      const unsigned dmy = lbase + NST * 16384 + 8192;
      const int wi = (idx - 28) * 4 + w;
      if (wi == 0) pf_line((unsigned)(xcd * 1024 + (lane & 7) * 128), gbuf, dmy);
      if (wi == 1) pf_line((unsigned)(xcd * 512 + (lane & 3) * 128), b2, dmy);
#pragma unroll
      for (int i = 0; i < 7; ++i)
        pf_line((unsigned)((i * 256 + wi * 16 + (lane >> 2)) * S_ + xcd * 256 + (lane & 3) * 64) * 2u, w2, dmy);
#pragma unroll
      for (int i = 0; i < 2; ++i) pf_line((unsigned)(xcd * 256 * D_ * 2 + (wi * 128 + i * 64 + lane) * 128), o, dmy);
    }
    f32x4 acca[2][2];
    gu32* fu = (gu32*)(flags + dt4 * FLAG_STRIDE + (lane < 28 ? lane : 27));
    unsigned vu = 0u;
    {
      zero_acc();
      auto rd = [&](int set) {
        const char* sb = smem + buf * 16384;
#pragma unroll
        for (int ks = 0; ks < 2; ++ks)
#pragma unroll
          for (int i = 0; i < 2; ++i) { fa[set][ks][i] = ld_tr2(sb + off_ks_r[i] + ks * 4096, 512); fb[set][ks][i] = ld_b128(sb + 8192 + wc * 4096 + off_kc[ks] + i * 2048); }
        nextbuf();
      };
      BAR();
      rd(0);
#pragma unroll 1
      for (int it = 0; it < 6; it += 2) {
        BAR();
        rd(1);
        mm(0);
        BAR();
        rd(0);
        if (it == PUB_IT && b < 224) {
          WAIT_V(0);
          if (lane == 0) {
            unsigned old = __hip_atomic_fetch_add(sync_words + 1, 1u, __ATOMIC_RELAXED, __HIP_MEMORY_SCOPE_WORKGROUP);
            if (old == 3u) __hip_atomic_store((gu32*)(flags + dt * FLAG_STRIDE + ft), 1u, __ATOMIC_RELAXED, __HIP_MEMORY_SCOPE_AGENT);
          }
        }
        mm(1);
      }
      if (wv == 0) vu = __hip_atomic_load(fu, __ATOMIC_RELAXED, __HIP_MEMORY_SCOPE_AGENT);
      BAR();
      rd(1);
      mm(0);
      mm(1);
#pragma unroll
      for (int i = 0; i < 2; ++i)
#pragma unroll
        for (int j = 0; j < 2; ++j) acca[i][j] = acc[i][j];
    }
    WAIT_L0();
    BAR();
    float gv[2], b2v[2];
#pragma unroll
    for (int j = 0; j < 2; ++j) {
      int s = s0 + wc * 32 + j * 16 + l15;
      gv[j] = gbuf[s];
      b2v[j] = lo16((unsigned)b2[s]);
    }
    if (wv == 0) {
      for (unsigned spin = 0; spin < (1u << 24); ++spin) {
        if (__builtin_amdgcn_ballot_w64(vu == 0u) == 0ull) break;
        __builtin_amdgcn_s_sleep(PSLP);
        vu = __hip_atomic_load(fu, __ATOMIC_RELAXED, __HIP_MEMORY_SCOPE_AGENT);
      }
    }
    BAR();
    {
      constexpr int NK = F_ / 64;
      zero_acc();
      buf = 0;
      auto rd = [&](int set) {
        const char* sb = smem + buf * 16384;
#pragma unroll
        for (int ks = 0; ks < 2; ++ks)
#pragma unroll
          for (int i = 0; i < 2; ++i) { fa[set][ks][i] = ld_b128(sb + 8192 + wr * 4096 + off_kc[ks] + i * 2048); fb[set][ks][i] = ld_tr2(sb + off_ks_c[i] + ks * 4096, 512); }
        nextbuf();
      };
      BAR();
      rd(0);
#pragma unroll 1
      for (int it = 0; it < NK - 2; it += 2) {
        BAR();
        ILV0();
        rd(1);
        mm(0);
        ILV(6, 2);
        BAR();
        ILV0();
        rd(0);
        mm(1);
        ILV(6, 2);
      }
      BAR();
      rd(1);
      mm(0);
      mm(1);
    }
#pragma unroll
    for (int j = 0; j < 2; ++j) {
      const int s = s0 + wc * 32 + j * 16 + l15;
      const float gg = gv[j], og = 1.f - gg;
      unsigned p[2][2];
#pragma unroll
      for (int i = 0; i < 2; ++i) {
        float r[4];
#pragma unroll
        for (int e = 0; e < 4; ++e) r[e] = gg * acca[i][j][e] + og * (acc[i][j][e] + b2v[j]);
        p[i][0] = pack2(r[0], r[1]); p[i][1] = pack2(r[2], r[3]);
      }
      auto q0 = __builtin_amdgcn_permlane16_swap(p[0][0], p[1][0], false, false);
      auto q1 = __builtin_amdgcn_permlane16_swap(p[0][1], p[1][1], false, false);
      u32x4 v = {(unsigned)q0[0], (unsigned)q1[0], (unsigned)q0[1], (unsigned)q1[1]};
      *(u32x4*)(out + (size_t)s * D_ + d0 + wr * 32 + (g & 1) * 16 + (g >> 1) * 8) = v;
    }
  }
}

extern "C" void kernel_launch(void* const* d_in, const int* in_sizes, int n_in, void* d_out, int out_size, void* d_ws,
                              size_t ws_size, hipStream_t stream) {
  const u16* x = (const u16*)d_in[0];
  const u16* wqkv = (const u16*)d_in[1];
  const u16* wo = (const u16*)d_in[2];
  const u16* w1 = (const u16*)d_in[4];
  const u16* b1 = (const u16*)d_in[5];
  const u16* w2 = (const u16*)d_in[6];
  const u16* b2 = (const u16*)d_in[7];
  const u16* gate_w = (const u16*)d_in[8];
  char* ws = (char*)d_ws;
  u16* obuf = (u16*)(ws);
  u16* hbuf = (u16*)(ws + (2 << 20));
  u16* ubuf = (u16*)(ws + (6 << 20));
  float* gbuf = (float*)(ws + (8 << 20));
  unsigned* flags = (unsigned*)(ws + (9 << 20));
  k1_qkv_attn_ln<<<256, 512, 0, stream>>>(x, wqkv, gate_w, obuf, hbuf, gbuf, w1, wo, flags);
  k2_mlp<<<256, 512, 0, stream>>>(hbuf, w1, b1, ubuf, obuf, wo, w2, b2, gbuf, (u16*)d_out, flags);
}
```

```cpp
#include <hip/hip_runtime.h>
#include <hip/hip_bf16.h>
#include <stdint.h>

typedef __attribute__((ext_vector_type(8))) short bf16x8;
typedef __attribute__((ext_vector_type(4))) short s16x4;
typedef __attribute__((ext_vector_type(4))) float f32x4;
typedef __attribute__((ext_vector_type(4))) unsigned u32x4;
typedef __attribute__((ext_vector_type(2))) unsigned u32x2;
typedef __attribute__((ext_vector_type(2))) __bf16 bf2;
typedef unsigned short u16;

#define AS1 __attribute__((address_space(1)))
#define AS3 __attribute__((address_space(3)))

constexpr int S_ = 2048, D_ = 512, F_ = 1792;

__device__ __forceinline__ void glds16(unsigned voff, const void* sbase, unsigned lds) {
  unsigned keep;
  lds = __builtin_amdgcn_readfirstlane(lds);
  asm volatile("s_mov_b32 %0, m0\n\ts_mov_b32 m0, %3\n\ts_nop 0\n\tglobal_load_lds_dwordx4 %1, %2\n\ts_mov_b32 m0, %0"
               : "=&s"(keep) : "v"(voff), "s"(sbase), "s"(lds) : "memory");
}
__device__ __forceinline__ unsigned lds_addr(const char* p) { return (unsigned)(size_t)(const AS3 char*)p; }
#define WAIT_V(n) asm volatile("s_waitcnt vmcnt(%0)" ::"n"(n) : "memory")
#define WAIT_L0() asm volatile("s_waitcnt lgkmcnt(0)" ::: "memory")
#define BAR() asm volatile("s_barrier" ::: "memory")

__device__ __forceinline__ unsigned pack2(float a, float b) {
  typedef __attribute__((ext_vector_type(2))) float f32x2;
  f32x2 t = {a, b};
  return __builtin_bit_cast(unsigned, __builtin_convertvector(t, bf2));
}
__device__ __forceinline__ float rbf(float a) { return (float)(__bf16)a; }
__device__ __forceinline__ float lo16(unsigned u) { return __builtin_bit_cast(float, u << 16); }
__device__ __forceinline__ float hi16(unsigned u) { return __builtin_bit_cast(float, u & 0xffff0000u); }

__device__ __forceinline__ bf16x8 ld_b128(const char* p) { return *(const AS3 bf16x8*)(p); }
__device__ __forceinline__ bf16x8 ld_tr2(const char* p, int t_off) {
  s16x4 lo = __builtin_amdgcn_ds_read_tr16_b64_v4i16((AS3 s16x4*)(p));
  s16x4 hi = __builtin_amdgcn_ds_read_tr16_b64_v4i16((AS3 s16x4*)(p + t_off));
  return __builtin_shufflevector(lo, hi, 0, 1, 2, 3, 4, 5, 6, 7);
}
#define MFMA(a, b, c) __builtin_amdgcn_mfma_f32_16x16x32_bf16(a, b, c, 0, 0, 0)

__device__ __forceinline__ void pf_line(unsigned voff, const void* sbase, unsigned lds) {
  unsigned keep;
  lds = __builtin_amdgcn_readfirstlane(lds);
  asm volatile("s_mov_b32 %0, m0\n\ts_mov_b32 m0, %3\n\ts_nop 0\n\tglobal_load_lds_dword %1, %2\n\ts_mov_b32 m0, %0"
               : "=&s"(keep) : "v"(voff), "s"(sbase), "s"(lds) : "memory");
}
__device__ __forceinline__ void glds16_sc1(unsigned voff, const void* sbase, unsigned lds) {
  unsigned keep;
  lds = __builtin_amdgcn_readfirstlane(lds);
  asm volatile("s_mov_b32 %0, m0\n\ts_mov_b32 m0, %3\n\ts_nop 0\n\tglobal_load_lds_dwordx4 %1, %2 sc1\n\ts_mov_b32 m0, %0"
               : "=&s"(keep) : "v"(voff), "s"(sbase), "s"(lds) : "memory");
}
__device__ __forceinline__ void st16_sc1(void* p, u32x4 v) {
  asm volatile("global_store_dwordx4 %0, %1, off sc1\n\ts_nop 1" ::"v"(p), "v"(v) : "memory");
}
typedef AS1 unsigned gu32;
constexpr int FLAG_STRIDE = 32;

__device__ __forceinline__ unsigned src_off_ks(int ins, int lane, int ld) {
  int r = ins * 8 + (lane >> 3);
  int h = ((r >> 1) & 1) | (((r >> 3) & 1) << 1);
  int ch = (lane & 7) ^ (2 * h);
  return (unsigned)(r * ld + ch * 8);
}
__device__ __forceinline__ unsigned src_off_kc(int ins, int lane, int ld) {
  int r = ins * 8 + (lane >> 3);
  int ch = (lane & 7) ^ ((r >> 1) & 7);
  return (unsigned)(r * ld + ch * 8);
}
__device__ __forceinline__ int frag_off_ks(int lane, int ct) {
  int g = lane >> 4, q = (lane & 15) >> 2, p = lane & 3;
  int h = ((q >> 1) & 1) | ((g & 1) << 1);
  return (8 * g + q) * 128 + (((ct ^ h) * 2 + (p >> 1)) * 16) + 8 * (p & 1);
}
__device__ __forceinline__ int frag_off_kc(int lane, int ks) {
  int g = lane >> 4, l15 = lane & 15;
  return l15 * 128 + (((ks * 4 + g) ^ (l15 >> 1)) * 16);
}

template <int CTRL> __device__ __forceinline__ float dpp_f(float v) {
  return __builtin_bit_cast(float, __builtin_amdgcn_update_dpp(0, __builtin_bit_cast(int, v), CTRL, 0xf, 0xf, true));
}
__device__ __forceinline__ void sw16(float v, float& a, float& b) {
  unsigned u = __builtin_bit_cast(unsigned, v);
  auto r = __builtin_amdgcn_permlane16_swap(u, u, false, false);
  a = __builtin_bit_cast(float, (unsigned)r[0]); b = __builtin_bit_cast(float, (unsigned)r[1]);
}
__device__ __forceinline__ void sw32(float v, float& a, float& b) {
  unsigned u = __builtin_bit_cast(unsigned, v);
  auto r = __builtin_amdgcn_permlane32_swap(u, u, false, false);
  a = __builtin_bit_cast(float, (unsigned)r[0]); b = __builtin_bit_cast(float, (unsigned)r[1]);
}
__device__ __forceinline__ float max3(float a, float b, float c) {
  float r;
  asm("v_max3_f32 %0, %1, %2, %3" : "=v"(r) : "v"(a), "v"(b), "v"(c));
  return r;
}
__device__ __forceinline__ float sum32(float v) {
  v += dpp_f<0xB1>(v);
  v += dpp_f<0x4E>(v);
  v += dpp_f<0x141>(v);
  v += dpp_f<0x140>(v);
  float a, b;
  sw16(v, a, b);
  return a + b;
}
#define SB0() __builtin_amdgcn_sched_barrier(0)
#define USE_ILV 1
#define ILV0() SB0()
#define ILV(n, r) do { interleave<n, r, 8 - n>(); SB0(); } while (0)
template <int N, int R, int M> __device__ __forceinline__ void interleave() {
#pragma unroll
  for (int i = 0; i < N; ++i) { __builtin_amdgcn_sched_group_barrier(0x008, 1, 0); __builtin_amdgcn_sched_group_barrier(0x100, R, 0); }
  if (M > 0) __builtin_amdgcn_sched_group_barrier(0x008, M, 0);
}
__device__ __forceinline__ float gelu_tanh(float v) {
  const float c1 = -2.f * 0.7978845608028654f * 1.4426950408889634f, c2 = c1 * 0.044715f;
  float e = __builtin_amdgcn_exp2f(v * __builtin_fmaf(v * v, c2, c1));
  return v * __builtin_amdgcn_rcpf(1.f + e);
}

constexpr int K1_STAGE = 34816;
constexpr int K1_NST = 4;
#define WOPF 1
#define CODEPF 1
#define CODEPF1 0
#define K1PF 5
#define MFMA16(a, b, c) __builtin_amdgcn_mfma_f32_16x16x16bf16_1k(a, b, c, 0, 0, 0)
__device__ __forceinline__ s16x4 pack4(f32x4 v) {
  u32x2 t = {pack2(v[0], v[1]), pack2(v[2], v[3])};
  return __builtin_bit_cast(s16x4, t);
}
extern "C" __global__ __launch_bounds__(512, 2) void k1_qkv_attn_ln(const u16* __restrict__ x, const u16* __restrict__ wqkv,
                                                         const u16* __restrict__ gate_w, u16* __restrict__ o,
                                                         u16* __restrict__ hbuf, float* __restrict__ gbuf,
                                                         const u16* __restrict__ w1, const u16* __restrict__ wo,
                                                         unsigned* __restrict__ flags) {
  __shared__ __attribute__((aligned(16))) char smem[K1_STAGE * K1_NST + 256 + 4096];
  const int tid = threadIdx.x, lane = tid & 63;
  const int wv = __builtin_amdgcn_readfirstlane(tid >> 6);
  const int w = wv & 3;
  const int b = blockIdx.x, xcd = b & 7, idx = b >> 3;
  const int rt = (xcd >> 1) * 8 + (idx & 7), hg = (xcd & 1) * 4 + (idx >> 3);
  const int s0 = rt * 64;
  const int g = lane >> 4, l15 = lane & 15;
  const unsigned lbase = lds_addr(smem);
  const int rot = ((idx & 7) + 2 * (idx >> 3)) & 7;

  if (wv >= 4) {
    unsigned gw[8];
    unsigned gp[9];
#pragma unroll
    for (int m = 0; m < 9; ++m) {
      int j = w + 4 * m;
      if (j < 24) {
        gp[m] = ((j >> 3) * D_ + hg * 64 + src_off_ks(j & 7, lane, 3 * D_)) * 2u;
      } else {
        int jj = j - 24;
        jj = jj > 9 ? 9 : jj;
        int r = jj * 8 + (lane >> 3);
        int ch = (lane & 7) ^ ((r >> 1) & 7);
        int srow = s0 - 16 + r;
        srow = srow < 0 ? 0 : srow;
        gp[m] = (unsigned)(srow * D_ + ch * 8) * 2u;
      }
    }
    auto issue = [&](int st) {
      const unsigned dst = lbase + (st % K1_NST) * K1_STAGE + w * 1024;
      const int sr = (st + rot) & 7;
      const u16* xa = x + sr * 64;
      const u16* wa = wqkv + (size_t)sr * 64 * 3 * D_;
      glds16(gp[6], xa, dst + 6 * 4096);
      glds16(gp[7], xa, dst + 7 * 4096);
      if (w < 2) glds16(gp[8], xa, dst + 8 * 4096);
#pragma unroll
      for (int m = 0; m < 6; ++m) glds16(gp[m], wa, dst + m * 4096);
    };
    const int lnr = 16 + hg * 8 + 2 * w + (lane >> 5);
    const int lnoff = 24576 + lnr * 128 + ((((lane & 31) >> 2) ^ ((lnr >> 1) & 7)) * 16) + (lane & 3) * 4;
    unsigned xs[8];
    issue(0); issue(1);
    glds16(lane * 16u, gate_w, lbase + K1_STAGE * K1_NST + 256 + w * 1024);
#pragma unroll
    for (int it = 0; it < 8; ++it) {
      constexpr int NI[9] = {2, 4, 5, 6, 7, 8, 8, 8, 8};
      const int ahead = NI[it] - it - 1;
      const int npf = it > K1PF ? 2 + WOPF + CODEPF : it < 2 ? 1 + CODEPF1 : 0;
      WAIT_L0();
      if (w < 2) { WAIT_V(ahead * 9 + npf); } else { WAIT_V(ahead * 8 + npf); }
      BAR();
#pragma unroll
      for (int st = NI[it]; st < NI[it + 1]; ++st) issue(st);
      if (it == K1PF) {
        const unsigned dmy = lbase + K1_STAGE * K1_NST;
        const int ln = (lane & 7) < 7 ? (lane & 7) : 6;
        const unsigned ro = (unsigned)((idx * 64 + w * 16 + (lane >> 3)) * F_ + (xcd >> 1) * 448) * 2u + ln * 128;
#pragma unroll
        for (int i = 0; i < 2; ++i) pf_line(ro + i * 8 * F_ * 2, w1, dmy);
        pf_line((unsigned)(idx * 128 + w * 32 + (lane & 31)) * 128u, wo, dmy);
        {
          unsigned long long pc;
          unsigned rel;
          asm volatile("s_getpc_b64 %0\n\ts_mov_b32 %1, k2_mlp@rel32@lo+4" : "=s"(pc), "=s"(rel));
          const int cl = w * 26 + (lane < 26 ? lane : 25);
          pf_line((unsigned)(cl < 100 ? cl : 100) * 128u, (const void*)(pc + (long long)(int)rel), dmy);
        }
      }
      xs[it] = *(const AS3 unsigned*)(smem + (it % K1_NST) * K1_STAGE + lnoff);
    }
    {
#pragma unroll
      for (int i = 0; i < 8; ++i) gw[i] = *(const AS3 unsigned*)(smem + K1_STAGE * K1_NST + 256 + w * 1024 + ((i + rot) & 7) * 128 + (lane & 31) * 4);
      float xv[16];
      float sum = 0.f, gs = 0.f;
#pragma unroll
      for (int i = 0; i < 8; ++i) {
        xv[2 * i] = lo16(xs[i]);
        xv[2 * i + 1] = hi16(xs[i]);
        sum += xv[2 * i] + xv[2 * i + 1];
        gs += xv[2 * i] * lo16(gw[i]) + xv[2 * i + 1] * hi16(gw[i]);
      }
      sum = sum32(sum);
      gs = sum32(gs);
      float mean = sum * (1.f / 512.f);
      float vs = 0.f;
#pragma unroll
      for (int i = 0; i < 16; ++i) { float d = xv[i] - mean; vs += d * d; }
      vs = sum32(vs);
      float rden = 1.f / sqrtf(vs * (1.f / 512.f) + 1e-5f);
      const int srow = s0 + hg * 8 + 2 * w + (lane >> 5);
#pragma unroll
      for (int i = 0; i < 8; ++i) {
        float a0 = (xv[2 * i] - mean) * rden, a1 = (xv[2 * i + 1] - mean) * rden;
        *(unsigned*)(hbuf + (size_t)srow * D_ + ((i + rot) & 7) * 64 + (lane & 31) * 2) = pack2(a0, a1);
      }
      if ((lane & 31) == 0) gbuf[srow] = 1.f / (1.f + __expf(-gs));
    }
    WAIT_V(0);
  } else {
    const int offw = frag_off_ks(lane, w);
    int xoff[2];
#pragma unroll
    for (int ks = 0; ks < 2; ++ks) xoff[ks] = 24576 + frag_off_kc(lane, ks);
    f32x4 aq[4], ak[5], av[5];
#pragma unroll
    for (int i = 0; i < 5; ++i) { ak[i] = f32x4{0.f, 0.f, 0.f, 0.f}; av[i] = f32x4{0.f, 0.f, 0.f, 0.f}; if (i < 4) aq[i] = f32x4{0.f, 0.f, 0.f, 0.f}; }
    bf16x8 fw[2][3], fx[2][5];
    auto rd = [&](int it, int ks) {
      const char* sb = smem + (it % K1_NST) * K1_STAGE;
#pragma unroll
      for (int m = 0; m < 3; ++m) fw[ks][m] = ld_tr2(sb + m * 8192 + offw + ks * 4096, 512);
#pragma unroll
      for (int r = 0; r < 5; ++r) fx[ks][r] = ld_b128(sb + xoff[ks] + r * 2048);
    };
    auto mm = [&](int ks) {
#pragma unroll
      for (int r = 0; r < 5; ++r) {
        if (r > 0) aq[r - 1] = MFMA(fw[ks][0], fx[ks][r], aq[r - 1]);
        ak[r] = MFMA(fw[ks][1], fx[ks][r], ak[r]);
        av[r] = MFMA(fx[ks][r], fw[ks][2], av[r]);
      }
    };
    BAR();
    if (b < 32) flags[b * 256 + tid] = 0u;
    rd(0, 0);
#pragma unroll 1
    for (int it = 0; it < 7; ++it) {
      SB0();
      rd(it, 1);
      mm(0);
      interleave<6, 2, 8>();
      SB0();
      WAIT_L0();
      BAR();
      SB0();
      rd(it + 1, 0);
      mm(1);
      interleave<6, 2, 8>();
    }
    SB0();
    rd(7, 1);
    mm(0);
    interleave<6, 2, 8>();
    SB0();
    mm(1);
    s16x4 kb[5], vb[5];
#pragma unroll
    for (int r = 0; r < 5; ++r) { kb[r] = pack4(ak[r]); vb[r] = pack4(av[r]); }
#pragma unroll
    for (int t = 0; t < 4; ++t) {
      s16x4 qb = pack4(aq[t]);
      f32x4 z = {0.f, 0.f, 0.f, 0.f};
      f32x4 sp = MFMA16(kb[t], qb, z);
      f32x4 sc = MFMA16(kb[t + 1], qb, z);
      float lg[8];
      float mx = -3.0e38f;
#pragma unroll
      for (int j = 0; j < 4; ++j) {
        const int dp = l15 + 16 - (4 * g + j), dc = l15 - (4 * g + j);
        const bool vp = (dp <= 14) && !(dp & 1) && (s0 + t * 16 + l15 - dp >= 0);
        const bool vc = (dc >= 0) && (dc <= 14) && !(dc & 1);
        lg[j] = vp ? sp[j] : -3.0e38f;
        lg[4 + j] = vc ? sc[j] : -3.0e38f;
      }
      mx = max3(max3(max3(lg[0], lg[1], lg[2]), lg[3], lg[4]), max3(lg[5], lg[6], lg[7]), mx);
      { float a, c; sw16(mx, a, c); mx = max3(a, c, c); sw32(mx, a, c); mx = max3(a, c, c); }
      float sum = 0.f;
      const float cs = 0.25f * 1.4426950408889634f, mc = -mx * cs;
#pragma unroll
      for (int j = 0; j < 8; ++j) { lg[j] = __builtin_amdgcn_exp2f(__builtin_fmaf(lg[j], cs, mc)); sum += lg[j]; }
      { float a, c; sw16(sum, a, c); sum = a + c; sw32(sum, a, c); sum = a + c; }
      const float rs = __builtin_amdgcn_rcpf(sum);
      s16x4 pp = pack4(f32x4{lg[0] * rs, lg[1] * rs, lg[2] * rs, lg[3] * rs});
      s16x4 pc = pack4(f32x4{lg[4] * rs, lg[5] * rs, lg[6] * rs, lg[7] * rs});
      f32x4 ot = MFMA16(vb[t], pp, z);
      ot = MFMA16(vb[t + 1], pc, ot);
      u32x2 v = {pack2(ot[0], ot[1]), pack2(ot[2], ot[3])};
      *(u32x2*)(o + (size_t)(s0 + t * 16 + l15) * D_ + (hg * 4 + w) * 16 + 4 * g) = v;
    }
  }
}

constexpr int NST = 9;
#define EARLY 1
#define GLATE 0
#define SECT 0
#define OPF 1
#define A0A 3
#define A0C 3
#define PIPEPOLL 0
#define FCOPY 32
#define EPOLL 1
#define PSLP 1
#define PUB_IT 0
#define ROTD 0

__device__ constexpr int n_issued(int j, int NK, int A0) {
  int a = A0 + 2 + 2 * j, c = (j - 1 + NST) > NST ? (j - 1 + NST) : NST;
  int m = a < c ? a : c;
  return m < NK ? m : NK;
}
__device__ constexpr int allowed(int done, int n, bool split) {
  int c = 0;
  for (int s = done + 1; s < n; ++s) c += (split && s < NST) ? 2 : 4;
  return c;
}

template <int NK, bool SPLIT, int A0, class ISSUE>
__device__ __forceinline__ void loader_stream(ISSUE issue) {
#pragma unroll
  for (int st = 0; st < n_issued(-1, NK, A0); ++st) issue(st, st, SPLIT);
  WAIT_V(allowed(0, n_issued(-1, NK, A0), SPLIT));
  BAR();
  constexpr int UN = (NST - 2) < (NK - 1) ? (NST - 2) : (NK - 1);
#pragma unroll
  for (int j = 0; j < UN; ++j) {
#pragma unroll
    for (int st = n_issued(j - 1, NK, A0); st < n_issued(j, NK, A0); ++st) issue(st, st % NST, SPLIT && st < NST);
    WAIT_V(allowed(j + 1, n_issued(j, NK, A0), SPLIT));
    BAR();
  }
  if constexpr (NK > NST) {
    int buf = (UN - 2) % NST;
#pragma unroll 1
    for (int it = UN; it < NK - NST + 2; ++it) {
      issue(it - 2 + NST, buf, false);
      buf = buf + 1 == NST ? 0 : buf + 1;
      WAIT_V((NST - 3) * 4);
      BAR();
    }
#pragma unroll
    for (int t = 0; t < NST - 3; ++t) {
      WAIT_V((NST - 4 - t) * 4);
      BAR();
    }
  }
}

extern "C" __global__ __launch_bounds__(512, 2) void k2_mlp(const u16* __restrict__ hbuf, const u16* __restrict__ w1,
                                                 const u16* __restrict__ b1, u16* ubuf,
                                                 const u16* __restrict__ o, const u16* __restrict__ wo,
                                                 const u16* __restrict__ w2,
                                                 const u16* __restrict__ b2, const float* __restrict__ gbuf,
                                                 u16* __restrict__ out, unsigned* flags) {
  __shared__ __attribute__((aligned(16))) char smem[NST * 16384 + 8192 + 512];
  const int tid = threadIdx.x, lane = tid & 63;
  const int wv = __builtin_amdgcn_readfirstlane(tid >> 6);
  const int w = wv & 3;
  const int b = blockIdx.x, xcd = b & 7, idx = b >> 3;
  const int g = lane >> 4, l15 = lane & 15;
  const unsigned lbase = lds_addr(smem);
  const int dt = (xcd & 1) * 4 + (idx & 3), ft = (xcd >> 1) * 7 + (idx >> 2);
  const int stl = xcd * 4 + (idx & 3), dt4 = idx >> 2;
  const int s0 = stl * 64, d0 = dt4 * 64;
  const int wr = w >> 1, wc = w & 1;
  AS3 unsigned* sync_words = (AS3 unsigned*)(smem + NST * 16384 + 8192 + 256);

  if (wv >= 4) {
    unsigned pw1[2], ph[2], pwo[2], po[2], pu[2], pw2[2];
#pragma unroll
    for (int i = 0; i < 2; ++i) {
      pw1[i] = (ft * 64 + src_off_ks(2 * w + i, lane, F_)) * 2u;
      ph[i] = (dt * 64 + src_off_ks(2 * w + i, lane, D_)) * 2u;
      pwo[i] = (d0 + src_off_ks(2 * w + i, lane, D_)) * 2u;
      po[i] = (s0 * D_ + src_off_kc(2 * w + i, lane, D_)) * 2u;
      pu[i] = (d0 * F_ + src_off_kc(2 * w + i, lane, F_)) * 2u;
      pw2[i] = (s0 + src_off_ks(2 * w + i, lane, S_)) * 2u;
    }
    const int rotA = (((idx >> 2) * 32) / 7 + ROTD * (idx & 3)) & 31;
    auto issueB = [&](int st, int buf) {
      const unsigned dst = lbase + buf * 16384 + w * 2048;
      const int sr = (st + dt4) & 7;
      const u16* a0 = wo + (size_t)sr * 64 * D_;
      const u16* a1 = o + sr * 64;
      glds16(pwo[0], a0, dst);
      glds16(pwo[1], a0, dst + 1024);
      glds16(po[0], a1, dst + 8192);
      glds16(po[1], a1, dst + 8192 + 1024);
    };
    if (b < 224) {
      loader_stream<40, false, A0A>([&](int st, int buf, bool) {
        if (st < 32) {
          const unsigned dst = lbase + buf * 16384 + w * 2048;
          const int sr = (st + rotA) & 31;
          const u16* a0 = w1 + (size_t)sr * 64 * F_;
          const u16* a1 = hbuf + (size_t)sr * 64 * D_;
          glds16(pw1[0], a0, dst);
          glds16(pw1[1], a0, dst + 1024);
          glds16(ph[0], a1, dst + 8192);
          glds16(ph[1], a1, dst + 8192 + 1024);
        } else {
          issueB(st - 32, buf);
        }
      });
    } else {
      loader_stream<8, false, 3>([&](int st, int buf, bool) { issueB(st, buf); });
    }
    BAR();
#pragma unroll
    for (int st = 0; st < (EARLY ? NST : 0); ++st) {
      const unsigned dst = lbase + st * 16384 + w * 2048;
      const int sr = st;
      const u16* a0 = w2 + (size_t)sr * 64 * S_;
      glds16(pw2[0], a0, dst);
      glds16(pw2[1], a0, dst + 1024);
    }
    BAR();
    loader_stream<F_ / 64, EARLY, A0C>([&](int st, int buf, bool only1) {
      const unsigned dst = lbase + buf * 16384 + w * 2048;
      const int sr = st;
      const u16* a0 = w2 + (size_t)sr * 64 * S_;
      const u16* a1 = ubuf + sr * 64;
      if (!only1) {
        glds16(pw2[0], a0, dst);
        glds16(pw2[1], a0, dst + 1024);
      }
      glds16_sc1(pu[0], a1, dst + 8192);
      glds16_sc1(pu[1], a1, dst + 8192 + 1024);
    });
  } else {
    if (tid < 2) sync_words[tid] = 0u;
    int off_ks_r[2], off_ks_c[2], off_kc[2];
#pragma unroll
    for (int i = 0; i < 2; ++i) { off_ks_r[i] = frag_off_ks(lane, wr * 2 + i); off_ks_c[i] = frag_off_ks(lane, wc * 2 + i); off_kc[i] = frag_off_kc(lane, i); }
    f32x4 acc[2][2];
    bf16x8 fa[2][2][2], fb[2][2][2];
    auto zero_acc = [&]() {
#pragma unroll
      for (int i = 0; i < 2; ++i)
#pragma unroll
        for (int j = 0; j < 2; ++j) acc[i][j] = f32x4{0.f, 0.f, 0.f, 0.f};
    };
    auto mm = [&](int set) {
#pragma unroll
      for (int ks = 0; ks < 2; ++ks)
#pragma unroll
        for (int i = 0; i < 2; ++i)
#pragma unroll
          for (int j = 0; j < 2; ++j) acc[i][j] = MFMA(fa[set][ks][i], fb[set][ks][j], acc[i][j]);
    };
    int buf = 0;
    auto nextbuf = [&]() { buf = buf + 1 == NST ? 0 : buf + 1; };
    if (b < 224) {
      const int dd0 = dt * 64, f0 = ft * 64;
      constexpr int NK = S_ / 64;
      u32x2 bb[2];
      {
        const unsigned* bp = (const unsigned*)(b1 + f0 + wr * 32);
        unsigned t[16];
#pragma unroll
        for (int i = 0; i < 16; ++i) t[i] = bp[i];
#pragma unroll
        for (int i = 0; i < 2; ++i)
#pragma unroll
          for (int k = 0; k < 2; ++k) {
            unsigned lo = (g & 1) ? t[i * 8 + 2 + k] : t[i * 8 + k];
            unsigned hi = (g & 1) ? t[i * 8 + 6 + k] : t[i * 8 + 4 + k];
            bb[i][k] = (g & 2) ? hi : lo;
          }
      }
      zero_acc();
      auto rd = [&](int set) {
        const char* sb = smem + buf * 16384;
#pragma unroll
        for (int ks = 0; ks < 2; ++ks)
#pragma unroll
          for (int i = 0; i < 2; ++i) { fa[set][ks][i] = ld_tr2(sb + off_ks_r[i] + ks * 4096, 512); fb[set][ks][i] = ld_tr2(sb + 8192 + off_ks_c[i] + ks * 4096, 512); }
        nextbuf();
      };
      BAR();
      rd(0);
#pragma unroll 1
      for (int it = 0; it < NK - 2; it += 2) {
        BAR();
        ILV0();
        rd(1);
        mm(0);
        ILV(8, 2);
        BAR();
        ILV0();
        rd(0);
        mm(1);
        ILV(8, 2);
      }
      BAR();
      rd(1);
      mm(0);
      mm(1);
      {
        float bv[2][4];
#pragma unroll
        for (int i = 0; i < 2; ++i) { bv[i][0] = lo16(bb[i][0]); bv[i][1] = hi16(bb[i][0]); bv[i][2] = lo16(bb[i][1]); bv[i][3] = hi16(bb[i][1]); }
#pragma unroll
        for (int j = 0; j < 2; ++j) {
          unsigned p[2][2];
#pragma unroll
          for (int i = 0; i < 2; ++i) {
            float r[4];
#pragma unroll
            for (int e = 0; e < 4; ++e) r[e] = gelu_tanh(acc[i][j][e] + bv[i][e]);
            p[i][0] = pack2(r[0], r[1]); p[i][1] = pack2(r[2], r[3]);
          }
          auto q0 = __builtin_amdgcn_permlane16_swap(p[0][0], p[1][0], false, false);
          auto q1 = __builtin_amdgcn_permlane16_swap(p[0][1], p[1][1], false, false);
          u32x4 v = {(unsigned)q0[0], (unsigned)q1[0], (unsigned)q0[1], (unsigned)q1[1]};
          const int row = wc * 32 + j * 16 + l15;
          st16_sc1(ubuf + (size_t)(dd0 + row) * F_ + f0 + wr * 32 + (g & 1) * 16 + (g >> 1) * 8, v);
        }
      }
    } else {
      const unsigned dmy = lbase + NST * 16384 + 8192;
      const int wi = (idx - 28) * 4 + w;
      if (wi == 0) pf_line((unsigned)(xcd * 1024 + (lane & 7) * 128), gbuf, dmy);
      if (wi == 1) pf_line((unsigned)(xcd * 512 + (lane & 3) * 128), b2, dmy);
#pragma unroll
      for (int i = 0; i < 7; ++i)
        pf_line((unsigned)((i * 256 + wi * 16 + (lane >> 2)) * S_ + xcd * 256 + (lane & 3) * 64) * 2u, w2, dmy);
#pragma unroll
      for (int i = 0; i < 2; ++i) pf_line((unsigned)(xcd * 256 * D_ * 2 + (wi * 128 + i * 64 + lane) * 128), o, dmy);
    }
    f32x4 acca[2][2];
    gu32* fu = (gu32*)(flags + (FCOPY == 32 ? stl : (xcd & (FCOPY - 1))) * 256 + dt4 * FLAG_STRIDE + (lane < 28 ? lane : 27));
    unsigned vu = 0u;
    {
      zero_acc();
      auto rd = [&](int set) {
        const char* sb = smem + buf * 16384;
#pragma unroll
        for (int ks = 0; ks < 2; ++ks)
#pragma unroll
          for (int i = 0; i < 2; ++i) { fa[set][ks][i] = ld_tr2(sb + off_ks_r[i] + ks * 4096, 512); fb[set][ks][i] = ld_b128(sb + 8192 + wc * 4096 + off_kc[ks] + i * 2048); }
        nextbuf();
      };
      BAR();
      rd(0);
#pragma unroll 1
      for (int it = 0; it < 6; it += 2) {
        BAR();
        rd(1);
        mm(0);
        BAR();
        rd(0);
        if (it == PUB_IT && b < 224) {
          WAIT_V(0);
          unsigned old = 0u;
          if (lane == 0) old = __hip_atomic_fetch_add(sync_words + 1, 1u, __ATOMIC_RELAXED, __HIP_MEMORY_SCOPE_WORKGROUP);
          old = __builtin_amdgcn_readfirstlane(old);
          if (old == 3u && lane < FCOPY) __hip_atomic_store((gu32*)(flags + lane * 256 + dt * FLAG_STRIDE + ft), 1u, __ATOMIC_RELAXED, __HIP_MEMORY_SCOPE_AGENT);
        }
        mm(1);
      }
      if (wv == 0) vu = __hip_atomic_load(fu, __ATOMIC_RELAXED, __HIP_MEMORY_SCOPE_AGENT);
      BAR();
      rd(1);
      mm(0);
      mm(1);
#pragma unroll
      for (int i = 0; i < 2; ++i)
#pragma unroll
        for (int j = 0; j < 2; ++j) acca[i][j] = acc[i][j];
    }
    WAIT_L0();
    BAR();
    float gv[2], b2v[2];
#pragma unroll
    for (int j = 0; j < 2; ++j) {
      int s = s0 + wc * 32 + j * 16 + l15;
      gv[j] = gbuf[s];
      b2v[j] = lo16((unsigned)b2[s]);
    }
    if (wv == 0) {
      for (unsigned spin = 0; spin < (1u << 24); ++spin) {
        if (__builtin_amdgcn_ballot_w64(vu == 0u) == 0ull) break;
        __builtin_amdgcn_s_sleep(PSLP);
        vu = __hip_atomic_load(fu, __ATOMIC_RELAXED, __HIP_MEMORY_SCOPE_AGENT);
      }
    }
    BAR();
    {
      constexpr int NK = F_ / 64;
      zero_acc();
      buf = 0;
      auto rd = [&](int set) {
        const char* sb = smem + buf * 16384;
#pragma unroll
        for (int ks = 0; ks < 2; ++ks)
#pragma unroll
          for (int i = 0; i < 2; ++i) { fa[set][ks][i] = ld_b128(sb + 8192 + wr * 4096 + off_kc[ks] + i * 2048); fb[set][ks][i] = ld_tr2(sb + off_ks_c[i] + ks * 4096, 512); }
        nextbuf();
      };
      BAR();
      rd(0);
#pragma unroll 1
      for (int it = 0; it < NK - 2; it += 2) {
        BAR();
        ILV0();
        rd(1);
        mm(0);
        ILV(6, 2);
        BAR();
        ILV0();
        rd(0);
        mm(1);
        ILV(6, 2);
      }
      BAR();
      rd(1);
      mm(0);
      mm(1);
    }
#pragma unroll
    for (int j = 0; j < 2; ++j) {
      const int s = s0 + wc * 32 + j * 16 + l15;
      const float gg = gv[j], og = 1.f - gg;
      unsigned p[2][2];
#pragma unroll
      for (int i = 0; i < 2; ++i) {
        float r[4];
#pragma unroll
        for (int e = 0; e < 4; ++e) r[e] = gg * acca[i][j][e] + og * (acc[i][j][e] + b2v[j]);
        p[i][0] = pack2(r[0], r[1]); p[i][1] = pack2(r[2], r[3]);
      }
      auto q0 = __builtin_amdgcn_permlane16_swap(p[0][0], p[1][0], false, false);
      auto q1 = __builtin_amdgcn_permlane16_swap(p[0][1], p[1][1], false, false);
      u32x4 v = {(unsigned)q0[0], (unsigned)q1[0], (unsigned)q0[1], (unsigned)q1[1]};
      *(u32x4*)(out + (size_t)s * D_ + d0 + wr * 32 + (g & 1) * 16 + (g >> 1) * 8) = v;
    }
  }
}

extern "C" void kernel_launch(void* const* d_in, const int* in_sizes, int n_in, void* d_out, int out_size, void* d_ws,
                              size_t ws_size, hipStream_t stream) {
  const u16* x = (const u16*)d_in[0];
  const u16* wqkv = (const u16*)d_in[1];
  const u16* wo = (const u16*)d_in[2];
  const u16* w1 = (const u16*)d_in[4];
  const u16* b1 = (const u16*)d_in[5];
  const u16* w2 = (const u16*)d_in[6];
  const u16* b2 = (const u16*)d_in[7];
  const u16* gate_w = (const u16*)d_in[8];
  char* ws = (char*)d_ws;
  u16* obuf = (u16*)(ws);
  u16* hbuf = (u16*)(ws + (2 << 20));
  u16* ubuf = (u16*)(ws + (6 << 20));
  float* gbuf = (float*)(ws + (8 << 20));
  unsigned* flags = (unsigned*)(ws + (9 << 20));
  k1_qkv_attn_ln<<<256, 512, 0, stream>>>(x, wqkv, gate_w, obuf, hbuf, gbuf, w1, wo, flags);
  k2_mlp<<<256, 512, 0, stream>>>(hbuf, w1, b1, ubuf, obuf, wo, w2, b2, gbuf, (u16*)d_out, flags);
}
```

```cpp
#include <hip/hip_runtime.h>
#include <hip/hip_bf16.h>
#include <stdint.h>

typedef __attribute__((ext_vector_type(8))) short bf16x8;
typedef __attribute__((ext_vector_type(4))) short s16x4;
typedef __attribute__((ext_vector_type(4))) float f32x4;
typedef __attribute__((ext_vector_type(4))) unsigned u32x4;
typedef __attribute__((ext_vector_type(2))) unsigned u32x2;
typedef __attribute__((ext_vector_type(2))) __bf16 bf2;
typedef unsigned short u16;

#define AS1 __attribute__((address_space(1)))
#define AS3 __attribute__((address_space(3)))

constexpr int S_ = 2048, D_ = 512, F_ = 1792;

__device__ __forceinline__ void glds16(unsigned voff, const void* sbase, unsigned lds) {
  unsigned keep;
  lds = __builtin_amdgcn_readfirstlane(lds);
  asm volatile("s_mov_b32 %0, m0\n\ts_mov_b32 m0, %3\n\ts_nop 0\n\tglobal_load_lds_dwordx4 %1, %2\n\ts_mov_b32 m0, %0"
               : "=&s"(keep) : "v"(voff), "s"(sbase), "s"(lds) : "memory");
}
__device__ __forceinline__ unsigned lds_addr(const char* p) { return (unsigned)(size_t)(const AS3 char*)p; }
#define WAIT_V(n) asm volatile("s_waitcnt vmcnt(%0)" ::"n"(n) : "memory")
#define WAIT_L0() asm volatile("s_waitcnt lgkmcnt(0)" ::: "memory")
#define BAR() asm volatile("s_barrier" ::: "memory")

__device__ __forceinline__ unsigned pack2(float a, float b) {
  typedef __attribute__((ext_vector_type(2))) float f32x2;
  f32x2 t = {a, b};
  return __builtin_bit_cast(unsigned, __builtin_convertvector(t, bf2));
}
__device__ __forceinline__ float rbf(float a) { return (float)(__bf16)a; }
__device__ __forceinline__ float lo16(unsigned u) { return __builtin_bit_cast(float, u << 16); }
__device__ __forceinline__ float hi16(unsigned u) { return __builtin_bit_cast(float, u & 0xffff0000u); }

__device__ __forceinline__ bf16x8 ld_b128(const char* p) { return *(const AS3 bf16x8*)(p); }
__device__ __forceinline__ bf16x8 ld_tr2(const char* p, int t_off) {
  s16x4 lo = __builtin_amdgcn_ds_read_tr16_b64_v4i16((AS3 s16x4*)(p));
  s16x4 hi = __builtin_amdgcn_ds_read_tr16_b64_v4i16((AS3 s16x4*)(p + t_off));
  return __builtin_shufflevector(lo, hi, 0, 1, 2, 3, 4, 5, 6, 7);
}
#define MFMA(a, b, c) __builtin_amdgcn_mfma_f32_16x16x32_bf16(a, b, c, 0, 0, 0)

__device__ __forceinline__ void pf_line(unsigned voff, const void* sbase, unsigned lds) {
  unsigned keep;
  lds = __builtin_amdgcn_readfirstlane(lds);
  asm volatile("s_mov_b32 %0, m0\n\ts_mov_b32 m0, %3\n\ts_nop 0\n\tglobal_load_lds_dword %1, %2\n\ts_mov_b32 m0, %0"
               : "=&s"(keep) : "v"(voff), "s"(sbase), "s"(lds) : "memory");
}
__device__ __forceinline__ void glds16_sc1(unsigned voff, const void* sbase, unsigned lds) {
  unsigned keep;
  lds = __builtin_amdgcn_readfirstlane(lds);
  asm volatile("s_mov_b32 %0, m0\n\ts_mov_b32 m0, %3\n\ts_nop 0\n\tglobal_load_lds_dwordx4 %1, %2 sc1\n\ts_mov_b32 m0, %0"
               : "=&s"(keep) : "v"(voff), "s"(sbase), "s"(lds) : "memory");
}
__device__ __forceinline__ void st16_sc1(void* p, u32x4 v) {
  asm volatile("global_store_dwordx4 %0, %1, off sc1\n\ts_nop 1" ::"v"(p), "v"(v) : "memory");
}
typedef AS1 unsigned gu32;
constexpr int FLAG_STRIDE = 32;

__device__ __forceinline__ unsigned src_off_ks(int ins, int lane, int ld) {
  int r = ins * 8 + (lane >> 3);
  int h = ((r >> 1) & 1) | (((r >> 3) & 1) << 1);
  int ch = (lane & 7) ^ (2 * h);
  return (unsigned)(r * ld + ch * 8);
}
__device__ __forceinline__ unsigned src_off_kc(int ins, int lane, int ld) {
  int r = ins * 8 + (lane >> 3);
  int ch = (lane & 7) ^ ((r >> 1) & 7);
  return (unsigned)(r * ld + ch * 8);
}
__device__ __forceinline__ int frag_off_ks(int lane, int ct) {
  int g = lane >> 4, q = (lane & 15) >> 2, p = lane & 3;
  int h = ((q >> 1) & 1) | ((g & 1) << 1);
  return (8 * g + q) * 128 + (((ct ^ h) * 2 + (p >> 1)) * 16) + 8 * (p & 1);
}
__device__ __forceinline__ int frag_off_kc(int lane, int ks) {
  int g = lane >> 4, l15 = lane & 15;
  return l15 * 128 + (((ks * 4 + g) ^ (l15 >> 1)) * 16);
}

template <int CTRL> __device__ __forceinline__ float dpp_f(float v) {
  return __builtin_bit_cast(float, __builtin_amdgcn_update_dpp(0, __builtin_bit_cast(int, v), CTRL, 0xf, 0xf, true));
}
__device__ __forceinline__ void sw16(float v, float& a, float& b) {
  unsigned u = __builtin_bit_cast(unsigned, v);
  auto r = __builtin_amdgcn_permlane16_swap(u, u, false, false);
  a = __builtin_bit_cast(float, (unsigned)r[0]); b = __builtin_bit_cast(float, (unsigned)r[1]);
}
__device__ __forceinline__ void sw32(float v, float& a, float& b) {
  unsigned u = __builtin_bit_cast(unsigned, v);
  auto r = __builtin_amdgcn_permlane32_swap(u, u, false, false);
  a = __builtin_bit_cast(float, (unsigned)r[0]); b = __builtin_bit_cast(float, (unsigned)r[1]);
}
__device__ __forceinline__ float max3(float a, float b, float c) {
  float r;
  asm("v_max3_f32 %0, %1, %2, %3" : "=v"(r) : "v"(a), "v"(b), "v"(c));
  return r;
}
__device__ __forceinline__ float sum32(float v) {
  v += dpp_f<0xB1>(v);
  v += dpp_f<0x4E>(v);
  v += dpp_f<0x141>(v);
  v += dpp_f<0x140>(v);
  float a, b;
  sw16(v, a, b);
  return a + b;
}
#define SB0() __builtin_amdgcn_sched_barrier(0)
#define USE_ILV 1
#define ILV0() SB0()
#define ILV(n, r) do { interleave<n, r, 8 - n>(); SB0(); } while (0)
template <int N, int R, int M> __device__ __forceinline__ void interleave() {
#pragma unroll
  for (int i = 0; i < N; ++i) { __builtin_amdgcn_sched_group_barrier(0x008, 1, 0); __builtin_amdgcn_sched_group_barrier(0x100, R, 0); }
  if (M > 0) __builtin_amdgcn_sched_group_barrier(0x008, M, 0);
}
__device__ __forceinline__ float gelu_tanh(float v) {
  const float c1 = -2.f * 0.7978845608028654f * 1.4426950408889634f, c2 = c1 * 0.044715f;
  float e = __builtin_amdgcn_exp2f(v * __builtin_fmaf(v * v, c2, c1));
  return v * __builtin_amdgcn_rcpf(1.f + e);
}

constexpr int K1_STAGE = 34816;
constexpr int K1_NST = 4;
#define WOPF 1
#define CODEPF 1
#define CODEPF1 0
#define K1PF 5
#define MFMA16(a, b, c) __builtin_amdgcn_mfma_f32_16x16x16bf16_1k(a, b, c, 0, 0, 0)
__device__ __forceinline__ s16x4 pack4(f32x4 v) {
  u32x2 t = {pack2(v[0], v[1]), pack2(v[2], v[3])};
  return __builtin_bit_cast(s16x4, t);
}
extern "C" __global__ __launch_bounds__(512, 2) void k1_qkv_attn_ln(const u16* __restrict__ x, const u16* __restrict__ wqkv,
                                                         const u16* __restrict__ gate_w, char* __restrict__ ws,
                                                         const u16* __restrict__ w1, const u16* __restrict__ wo) {
  u16* o = (u16*)ws;
  u16* hbuf = (u16*)(ws + (2 << 20));
  float* gbuf = (float*)(ws + (8 << 20));
  unsigned* flags = (unsigned*)(ws + (9 << 20));
  __shared__ __attribute__((aligned(16))) char smem[K1_STAGE * K1_NST + 256 + 4096];
  const int tid = threadIdx.x, lane = tid & 63;
  const int wv = __builtin_amdgcn_readfirstlane(tid >> 6);
  const int w = wv & 3;
  const int b = blockIdx.x, xcd = b & 7, idx = b >> 3;
  const int rt = (xcd >> 1) * 8 + (idx & 7), hg = (xcd & 1) * 4 + (idx >> 3);
  const int s0 = rt * 64;
  const int g = lane >> 4, l15 = lane & 15;
  const unsigned lbase = lds_addr(smem);
  const int rot = ((idx & 7) + 2 * (idx >> 3)) & 7;

  if (wv < 4) {
    const int offw = frag_off_ks(lane, w);
    int xoff[2];
#pragma unroll
    for (int ks = 0; ks < 2; ++ks) xoff[ks] = 24576 + frag_off_kc(lane, ks);
    f32x4 aq[4], ak[5], av[5];
#pragma unroll
    for (int i = 0; i < 5; ++i) { ak[i] = f32x4{0.f, 0.f, 0.f, 0.f}; av[i] = f32x4{0.f, 0.f, 0.f, 0.f}; if (i < 4) aq[i] = f32x4{0.f, 0.f, 0.f, 0.f}; }
    bf16x8 fw[2][3], fx[2][5];
    auto rd = [&](int it, int ks) {
      const char* sb = smem + (it % K1_NST) * K1_STAGE;
#pragma unroll
      for (int m = 0; m < 3; ++m) fw[ks][m] = ld_tr2(sb + m * 8192 + offw + ks * 4096, 512);
#pragma unroll
      for (int r = 0; r < 5; ++r) fx[ks][r] = ld_b128(sb + xoff[ks] + r * 2048);
    };
    auto mm = [&](int ks) {
#pragma unroll
      for (int r = 0; r < 5; ++r) {
        if (r > 0) aq[r - 1] = MFMA(fw[ks][0], fx[ks][r], aq[r - 1]);
        ak[r] = MFMA(fw[ks][1], fx[ks][r], ak[r]);
        av[r] = MFMA(fx[ks][r], fw[ks][2], av[r]);
      }
    };
    BAR();
    if (b < 32) flags[b * 256 + tid] = 0u;
    rd(0, 0);
#pragma unroll 1
    for (int it = 0; it < 7; ++it) {
      SB0();
      rd(it, 1);
      mm(0);
      interleave<6, 2, 8>();
      SB0();
      WAIT_L0();
      BAR();
      SB0();
      rd(it + 1, 0);
      mm(1);
      interleave<6, 2, 8>();
    }
    SB0();
    rd(7, 1);
    mm(0);
    interleave<6, 2, 8>();
    SB0();
    mm(1);
    s16x4 kb[5], vb[5];
#pragma unroll
    for (int r = 0; r < 5; ++r) { kb[r] = pack4(ak[r]); vb[r] = pack4(av[r]); }
#pragma unroll
    for (int t = 0; t < 4; ++t) {
      s16x4 qb = pack4(aq[t]);
      f32x4 z = {0.f, 0.f, 0.f, 0.f};
      f32x4 sp = MFMA16(kb[t], qb, z);
      f32x4 sc = MFMA16(kb[t + 1], qb, z);
      float lg[8];
      float mx = -3.0e38f;
#pragma unroll
      for (int j = 0; j < 4; ++j) {
        const int dp = l15 + 16 - (4 * g + j), dc = l15 - (4 * g + j);
        const bool vp = (dp <= 14) && !(dp & 1) && (s0 + t * 16 + l15 - dp >= 0);
        const bool vc = (dc >= 0) && (dc <= 14) && !(dc & 1);
        lg[j] = vp ? sp[j] : -3.0e38f;
        lg[4 + j] = vc ? sc[j] : -3.0e38f;
      }
      mx = max3(max3(max3(lg[0], lg[1], lg[2]), lg[3], lg[4]), max3(lg[5], lg[6], lg[7]), mx);
      { float a, c; sw16(mx, a, c); mx = max3(a, c, c); sw32(mx, a, c); mx = max3(a, c, c); }
      float sum = 0.f;
      const float cs = 0.25f * 1.4426950408889634f, mc = -mx * cs;
#pragma unroll
      for (int j = 0; j < 8; ++j) { lg[j] = __builtin_amdgcn_exp2f(__builtin_fmaf(lg[j], cs, mc)); sum += lg[j]; }
      { float a, c; sw16(sum, a, c); sum = a + c; sw32(sum, a, c); sum = a + c; }
      const float rs = __builtin_amdgcn_rcpf(sum);
      s16x4 pp = pack4(f32x4{lg[0] * rs, lg[1] * rs, lg[2] * rs, lg[3] * rs});
      s16x4 pc = pack4(f32x4{lg[4] * rs, lg[5] * rs, lg[6] * rs, lg[7] * rs});
      f32x4 ot = MFMA16(vb[t], pp, z);
      ot = MFMA16(vb[t + 1], pc, ot);
      u32x2 v = {pack2(ot[0], ot[1]), pack2(ot[2], ot[3])};
      *(u32x2*)(o + (size_t)(s0 + t * 16 + l15) * D_ + (hg * 4 + w) * 16 + 4 * g) = v;
    }
  } else {
    unsigned gw[8];
    unsigned gp[9];
#pragma unroll
    for (int m = 0; m < 9; ++m) {
      int j = w + 4 * m;
      if (j < 24) {
        gp[m] = ((j >> 3) * D_ + hg * 64 + src_off_ks(j & 7, lane, 3 * D_)) * 2u;
      } else {
        int jj = j - 24;
        jj = jj > 9 ? 9 : jj;
        int r = jj * 8 + (lane >> 3);
        int ch = (lane & 7) ^ ((r >> 1) & 7);
        int srow = s0 - 16 + r;
        srow = srow < 0 ? 0 : srow;
        gp[m] = (unsigned)(srow * D_ + ch * 8) * 2u;
      }
    }
    auto issue = [&](int st) {
      const unsigned dst = lbase + (st % K1_NST) * K1_STAGE + w * 1024;
      const int sr = (st + rot) & 7;
      const u16* xa = x + sr * 64;
      const u16* wa = wqkv + (size_t)sr * 64 * 3 * D_;
      glds16(gp[6], xa, dst + 6 * 4096);
      glds16(gp[7], xa, dst + 7 * 4096);
      if (w < 2) glds16(gp[8], xa, dst + 8 * 4096);
#pragma unroll
      for (int m = 0; m < 6; ++m) glds16(gp[m], wa, dst + m * 4096);
    };
    const int lnr = 16 + hg * 8 + 2 * w + (lane >> 5);
    const int lnoff = 24576 + lnr * 128 + ((((lane & 31) >> 2) ^ ((lnr >> 1) & 7)) * 16) + (lane & 3) * 4;
    unsigned xs[8];
    issue(0); issue(1);
    glds16(lane * 16u, gate_w, lbase + K1_STAGE * K1_NST + 256 + w * 1024);
#pragma unroll
    for (int it = 0; it < 8; ++it) {
      constexpr int NI[9] = {2, 4, 5, 6, 7, 8, 8, 8, 8};
      const int ahead = NI[it] - it - 1;
      const int npf = it > K1PF ? 2 + WOPF + CODEPF : it < 2 ? 1 + CODEPF1 : 0;
      WAIT_L0();
      if (w < 2) { WAIT_V(ahead * 9 + npf); } else { WAIT_V(ahead * 8 + npf); }
      BAR();
#pragma unroll
      for (int st = NI[it]; st < NI[it + 1]; ++st) issue(st);
      if (it == K1PF) {
        const unsigned dmy = lbase + K1_STAGE * K1_NST;
        const int ln = (lane & 7) < 7 ? (lane & 7) : 6;
        const unsigned ro = (unsigned)((idx * 64 + w * 16 + (lane >> 3)) * F_ + (xcd >> 1) * 448) * 2u + ln * 128;
#pragma unroll
        for (int i = 0; i < 2; ++i) pf_line(ro + i * 8 * F_ * 2, w1, dmy);
        pf_line((unsigned)(idx * 128 + w * 32 + (lane & 31)) * 128u, wo, dmy);
        {
          unsigned long long pc;
          unsigned rel;
          asm volatile("s_getpc_b64 %0\n\ts_mov_b32 %1, k2_mlp@rel32@lo+4" : "=s"(pc), "=s"(rel));
          const int cl = w * 26 + (lane < 26 ? lane : 25);
          pf_line((unsigned)(cl < 100 ? cl : 100) * 128u, (const void*)(pc + (long long)(int)rel), dmy);
        }
      }
      xs[it] = *(const AS3 unsigned*)(smem + (it % K1_NST) * K1_STAGE + lnoff);
    }
    {
#pragma unroll
      for (int i = 0; i < 8; ++i) gw[i] = *(const AS3 unsigned*)(smem + K1_STAGE * K1_NST + 256 + w * 1024 + ((i + rot) & 7) * 128 + (lane & 31) * 4);
      float xv[16];
      float sum = 0.f, gs = 0.f;
#pragma unroll
      for (int i = 0; i < 8; ++i) {
        xv[2 * i] = lo16(xs[i]);
        xv[2 * i + 1] = hi16(xs[i]);
        sum += xv[2 * i] + xv[2 * i + 1];
        gs += xv[2 * i] * lo16(gw[i]) + xv[2 * i + 1] * hi16(gw[i]);
      }
      sum = sum32(sum);
      gs = sum32(gs);
      float mean = sum * (1.f / 512.f);
      float vs = 0.f;
#pragma unroll
      for (int i = 0; i < 16; ++i) { float d = xv[i] - mean; vs += d * d; }
      vs = sum32(vs);
      float rden = 1.f / sqrtf(vs * (1.f / 512.f) + 1e-5f);
      const int srow = s0 + hg * 8 + 2 * w + (lane >> 5);
#pragma unroll
      for (int i = 0; i < 8; ++i) {
        float a0 = (xv[2 * i] - mean) * rden, a1 = (xv[2 * i + 1] - mean) * rden;
        *(unsigned*)(hbuf + (size_t)srow * D_ + ((i + rot) & 7) * 64 + (lane & 31) * 2) = pack2(a0, a1);
      }
      if ((lane & 31) == 0) gbuf[srow] = 1.f / (1.f + __expf(-gs));
    }
    WAIT_V(0);
  }
}

constexpr int NST = 9;
#define EARLY 1
#define GLATE 0
#define SECT 0
#define OPF 1
#define A0A 3
#define A0C 3
#define PIPEPOLL 0
#define FCOPY 32
#define EPOLL 1
#define PSLP 1
#define PUB_IT 0
#define ROTD 0

__device__ constexpr int n_issued(int j, int NK, int A0) {
  int a = A0 + 2 + 2 * j, c = (j - 1 + NST) > NST ? (j - 1 + NST) : NST;
  int m = a < c ? a : c;
  return m < NK ? m : NK;
}
__device__ constexpr int allowed(int done, int n, bool split) {
  int c = 0;
  for (int s = done + 1; s < n; ++s) c += (split && s < NST) ? 2 : 4;
  return c;
}

template <int NK, bool SPLIT, int A0, class ISSUE>
__device__ __forceinline__ void loader_stream(ISSUE issue) {
#pragma unroll
  for (int st = 0; st < n_issued(-1, NK, A0); ++st) issue(st, st, SPLIT);
  WAIT_V(allowed(0, n_issued(-1, NK, A0), SPLIT));
  BAR();
  constexpr int UN = (NST - 2) < (NK - 1) ? (NST - 2) : (NK - 1);
#pragma unroll
  for (int j = 0; j < UN; ++j) {
#pragma unroll
    for (int st = n_issued(j - 1, NK, A0); st < n_issued(j, NK, A0); ++st) issue(st, st % NST, SPLIT && st < NST);
    WAIT_V(allowed(j + 1, n_issued(j, NK, A0), SPLIT));
    BAR();
  }
  if constexpr (NK > NST) {
    int buf = (UN - 2) % NST;
#pragma unroll 1
    for (int it = UN; it < NK - NST + 2; ++it) {
      issue(it - 2 + NST, buf, false);
      buf = buf + 1 == NST ? 0 : buf + 1;
      WAIT_V((NST - 3) * 4);
      BAR();
    }
#pragma unroll
    for (int t = 0; t < NST - 3; ++t) {
      WAIT_V((NST - 4 - t) * 4);
      BAR();
    }
  }
}

extern "C" __global__ __launch_bounds__(512, 2) void k2_mlp(const u16* __restrict__ hbuf, const u16* __restrict__ w1,
                                                 const u16* __restrict__ b1, u16* ubuf,
                                                 const u16* __restrict__ o, const u16* __restrict__ wo,
                                                 const u16* __restrict__ w2,
                                                 const u16* __restrict__ b2, const float* __restrict__ gbuf,
                                                 u16* __restrict__ out, unsigned* flags) {
  __shared__ __attribute__((aligned(16))) char smem[NST * 16384 + 8192 + 512];
  const int tid = threadIdx.x, lane = tid & 63;
  const int wv = __builtin_amdgcn_readfirstlane(tid >> 6);
  const int w = wv & 3;
  const int b = blockIdx.x, xcd = b & 7, idx = b >> 3;
  const int g = lane >> 4, l15 = lane & 15;
  const unsigned lbase = lds_addr(smem);
  const int dt = (xcd & 1) * 4 + (idx & 3), ft = (xcd >> 1) * 7 + (idx >> 2);
  const int stl = xcd * 4 + (idx & 3), dt4 = idx >> 2;
  const int s0 = stl * 64, d0 = dt4 * 64;
  const int wr = w >> 1, wc = w & 1;
  AS3 unsigned* sync_words = (AS3 unsigned*)(smem + NST * 16384 + 8192 + 256);

  if (wv < 4) {
    if (tid < 2) sync_words[tid] = 0u;
    int off_ks_r[2], off_ks_c[2], off_kc[2];
#pragma unroll
    for (int i = 0; i < 2; ++i) { off_ks_r[i] = frag_off_ks(lane, wr * 2 + i); off_ks_c[i] = frag_off_ks(lane, wc * 2 + i); off_kc[i] = frag_off_kc(lane, i); }
    f32x4 acc[2][2];
    bf16x8 fa[2][2][2], fb[2][2][2];
    auto zero_acc = [&]() {
#pragma unroll
      for (int i = 0; i < 2; ++i)
#pragma unroll
        for (int j = 0; j < 2; ++j) acc[i][j] = f32x4{0.f, 0.f, 0.f, 0.f};
    };
    auto mm = [&](int set) {
#pragma unroll
      for (int ks = 0; ks < 2; ++ks)
#pragma unroll
        for (int i = 0; i < 2; ++i)
#pragma unroll
          for (int j = 0; j < 2; ++j) acc[i][j] = MFMA(fa[set][ks][i], fb[set][ks][j], acc[i][j]);
    };
    int buf = 0;
    auto nextbuf = [&]() { buf = buf + 1 == NST ? 0 : buf + 1; };
    if (b < 224) {
      const int dd0 = dt * 64, f0 = ft * 64;
      constexpr int NK = S_ / 64;
      u32x2 bb[2];
      {
        const unsigned* bp = (const unsigned*)(b1 + f0 + wr * 32);
        unsigned t[16];
#pragma unroll
        for (int i = 0; i < 16; ++i) t[i] = bp[i];
#pragma unroll
        for (int i = 0; i < 2; ++i)
#pragma unroll
          for (int k = 0; k < 2; ++k) {
            unsigned lo = (g & 1) ? t[i * 8 + 2 + k] : t[i * 8 + k];
            unsigned hi = (g & 1) ? t[i * 8 + 6 + k] : t[i * 8 + 4 + k];
            bb[i][k] = (g & 2) ? hi : lo;
          }
      }
      zero_acc();
      auto rd = [&](int set) {
        const char* sb = smem + buf * 16384;
#pragma unroll
        for (int ks = 0; ks < 2; ++ks)
#pragma unroll
          for (int i = 0; i < 2; ++i) { fa[set][ks][i] = ld_tr2(sb + off_ks_r[i] + ks * 4096, 512); fb[set][ks][i] = ld_tr2(sb + 8192 + off_ks_c[i] + ks * 4096, 512); }
        nextbuf();
      };
      BAR();
      rd(0);
#pragma unroll 1
      for (int it = 0; it < NK - 2; it += 2) {
        BAR();
        ILV0();
        rd(1);
        mm(0);
        ILV(8, 2);
        BAR();
        ILV0();
        rd(0);
        mm(1);
        ILV(8, 2);
      }
      BAR();
      rd(1);
      mm(0);
      mm(1);
      {
        float bv[2][4];
#pragma unroll
        for (int i = 0; i < 2; ++i) { bv[i][0] = lo16(bb[i][0]); bv[i][1] = hi16(bb[i][0]); bv[i][2] = lo16(bb[i][1]); bv[i][3] = hi16(bb[i][1]); }
#pragma unroll
        for (int j = 0; j < 2; ++j) {
          unsigned p[2][2];
#pragma unroll
          for (int i = 0; i < 2; ++i) {
            float r[4];
#pragma unroll
            for (int e = 0; e < 4; ++e) r[e] = gelu_tanh(acc[i][j][e] + bv[i][e]);
            p[i][0] = pack2(r[0], r[1]); p[i][1] = pack2(r[2], r[3]);
          }
          auto q0 = __builtin_amdgcn_permlane16_swap(p[0][0], p[1][0], false, false);
          auto q1 = __builtin_amdgcn_permlane16_swap(p[0][1], p[1][1], false, false);
          u32x4 v = {(unsigned)q0[0], (unsigned)q1[0], (unsigned)q0[1], (unsigned)q1[1]};
          const int row = wc * 32 + j * 16 + l15;
          st16_sc1(ubuf + (size_t)(dd0 + row) * F_ + f0 + wr * 32 + (g & 1) * 16 + (g >> 1) * 8, v);
        }
      }
    } else {
      const unsigned dmy = lbase + NST * 16384 + 8192;
      const int wi = (idx - 28) * 4 + w;
      if (wi == 0) pf_line((unsigned)(xcd * 1024 + (lane & 7) * 128), gbuf, dmy);
      if (wi == 1) pf_line((unsigned)(xcd * 512 + (lane & 3) * 128), b2, dmy);
#pragma unroll
      for (int i = 0; i < 7; ++i)
        pf_line((unsigned)((i * 256 + wi * 16 + (lane >> 2)) * S_ + xcd * 256 + (lane & 3) * 64) * 2u, w2, dmy);
#pragma unroll
      for (int i = 0; i < 2; ++i) pf_line((unsigned)(xcd * 256 * D_ * 2 + (wi * 128 + i * 64 + lane) * 128), o, dmy);
    }
    f32x4 acca[2][2];
    gu32* fu = (gu32*)(flags + (FCOPY == 32 ? stl : (xcd & (FCOPY - 1))) * 256 + dt4 * FLAG_STRIDE + (lane < 28 ? lane : 27));
    unsigned vu = 0u;
    {
      zero_acc();
      auto rd = [&](int set) {
        const char* sb = smem + buf * 16384;
#pragma unroll
        for (int ks = 0; ks < 2; ++ks)
#pragma unroll
          for (int i = 0; i < 2; ++i) { fa[set][ks][i] = ld_tr2(sb + off_ks_r[i] + ks * 4096, 512); fb[set][ks][i] = ld_b128(sb + 8192 + wc * 4096 + off_kc[ks] + i * 2048); }
        nextbuf();
      };
      BAR();
      rd(0);
#pragma unroll 1
      for (int it = 0; it < 6; it += 2) {
        BAR();
        rd(1);
        mm(0);
        BAR();
        rd(0);
        if (it == PUB_IT && b < 224) {
          WAIT_V(0);
          unsigned old = 0u;
          if (lane == 0) old = __hip_atomic_fetch_add(sync_words + 1, 1u, __ATOMIC_RELAXED, __HIP_MEMORY_SCOPE_WORKGROUP);
          old = __builtin_amdgcn_readfirstlane(old);
          if (old == 3u && lane < FCOPY) __hip_atomic_store((gu32*)(flags + lane * 256 + dt * FLAG_STRIDE + ft), 1u, __ATOMIC_RELAXED, __HIP_MEMORY_SCOPE_AGENT);
        }
        mm(1);
      }
      if (wv == 0) vu = __hip_atomic_load(fu, __ATOMIC_RELAXED, __HIP_MEMORY_SCOPE_AGENT);
      BAR();
      rd(1);
      mm(0);
      mm(1);
#pragma unroll
      for (int i = 0; i < 2; ++i)
#pragma unroll
        for (int j = 0; j < 2; ++j) acca[i][j] = acc[i][j];
    }
    WAIT_L0();
    BAR();
    float gv[2], b2v[2];
#pragma unroll
    for (int j = 0; j < 2; ++j) {
      int s = s0 + wc * 32 + j * 16 + l15;
      gv[j] = gbuf[s];
      b2v[j] = lo16((unsigned)b2[s]);
    }
    if (wv == 0) {
      for (unsigned spin = 0; spin < (1u << 24); ++spin) {
        if (__builtin_amdgcn_ballot_w64(vu == 0u) == 0ull) break;
        __builtin_amdgcn_s_sleep(PSLP);
        vu = __hip_atomic_load(fu, __ATOMIC_RELAXED, __HIP_MEMORY_SCOPE_AGENT);
      }
    }
    BAR();
    {
      constexpr int NK = F_ / 64;
      zero_acc();
      buf = 0;
      auto rd = [&](int set) {
        const char* sb = smem + buf * 16384;
#pragma unroll
        for (int ks = 0; ks < 2; ++ks)
#pragma unroll
          for (int i = 0; i < 2; ++i) { fa[set][ks][i] = ld_b128(sb + 8192 + wr * 4096 + off_kc[ks] + i * 2048); fb[set][ks][i] = ld_tr2(sb + off_ks_c[i] + ks * 4096, 512); }
        nextbuf();
      };
      BAR();
      rd(0);
#pragma unroll 1
      for (int it = 0; it < NK - 2; it += 2) {
        BAR();
        ILV0();
        rd(1);
        mm(0);
        ILV(6, 2);
        BAR();
        ILV0();
        rd(0);
        mm(1);
        ILV(6, 2);
      }
      BAR();
      rd(1);
      mm(0);
      mm(1);
    }
#pragma unroll
    for (int j = 0; j < 2; ++j) {
      const int s = s0 + wc * 32 + j * 16 + l15;
      const float gg = gv[j], og = 1.f - gg;
      unsigned p[2][2];
#pragma unroll
      for (int i = 0; i < 2; ++i) {
        float r[4];
#pragma unroll
        for (int e = 0; e < 4; ++e) r[e] = gg * acca[i][j][e] + og * (acc[i][j][e] + b2v[j]);
        p[i][0] = pack2(r[0], r[1]); p[i][1] = pack2(r[2], r[3]);
      }
      auto q0 = __builtin_amdgcn_permlane16_swap(p[0][0], p[1][0], false, false);
      auto q1 = __builtin_amdgcn_permlane16_swap(p[0][1], p[1][1], false, false);
      u32x4 v = {(unsigned)q0[0], (unsigned)q1[0], (unsigned)q0[1], (unsigned)q1[1]};
      *(u32x4*)(out + (size_t)s * D_ + d0 + wr * 32 + (g & 1) * 16 + (g >> 1) * 8) = v;
    }
  } else {
    unsigned pw1[2], ph[2], pwo[2], po[2], pu[2], pw2[2];
#pragma unroll
    for (int i = 0; i < 2; ++i) {
      pw1[i] = (ft * 64 + src_off_ks(2 * w + i, lane, F_)) * 2u;
      ph[i] = (dt * 64 + src_off_ks(2 * w + i, lane, D_)) * 2u;
      pwo[i] = (d0 + src_off_ks(2 * w + i, lane, D_)) * 2u;
      po[i] = (s0 * D_ + src_off_kc(2 * w + i, lane, D_)) * 2u;
      pu[i] = (d0 * F_ + src_off_kc(2 * w + i, lane, F_)) * 2u;
      pw2[i] = (s0 + src_off_ks(2 * w + i, lane, S_)) * 2u;
    }
    const int rotA = (((idx >> 2) * 32) / 7 + ROTD * (idx & 3)) & 31;
    auto issueB = [&](int st, int buf) {
      const unsigned dst = lbase + buf * 16384 + w * 2048;
      const int sr = (st + dt4) & 7;
      const u16* a0 = wo + (size_t)sr * 64 * D_;
      const u16* a1 = o + sr * 64;
      glds16(pwo[0], a0, dst);
      glds16(pwo[1], a0, dst + 1024);
      glds16(po[0], a1, dst + 8192);
      glds16(po[1], a1, dst + 8192 + 1024);
    };
    if (b >= 224) {
      loader_stream<8, false, 3>([&](int st, int buf, bool) { issueB(st, buf); });
    } else {
      loader_stream<40, false, A0A>([&](int st, int buf, bool) {
        if (st < 32) {
          const unsigned dst = lbase + buf * 16384 + w * 2048;
          const int sr = (st + rotA) & 31;
          const u16* a0 = w1 + (size_t)sr * 64 * F_;
          const u16* a1 = hbuf + (size_t)sr * 64 * D_;
          glds16(pw1[0], a0, dst);
          glds16(pw1[1], a0, dst + 1024);
          glds16(ph[0], a1, dst + 8192);
          glds16(ph[1], a1, dst + 8192 + 1024);
        } else {
          issueB(st - 32, buf);
        }
      });
    }
    BAR();
#pragma unroll
    for (int st = 0; st < (EARLY ? NST : 0); ++st) {
      const unsigned dst = lbase + st * 16384 + w * 2048;
      const int sr = st;
      const u16* a0 = w2 + (size_t)sr * 64 * S_;
      glds16(pw2[0], a0, dst);
      glds16(pw2[1], a0, dst + 1024);
    }
    BAR();
    loader_stream<F_ / 64, EARLY, A0C>([&](int st, int buf, bool only1) {
      const unsigned dst = lbase + buf * 16384 + w * 2048;
      const int sr = st;
      const u16* a0 = w2 + (size_t)sr * 64 * S_;
      const u16* a1 = ubuf + sr * 64;
      if (!only1) {
        glds16(pw2[0], a0, dst);
        glds16(pw2[1], a0, dst + 1024);
      }
      glds16_sc1(pu[0], a1, dst + 8192);
      glds16_sc1(pu[1], a1, dst + 8192 + 1024);
    });
  }
}

extern "C" void kernel_launch(void* const* d_in, const int* in_sizes, int n_in, void* d_out, int out_size, void* d_ws,
                              size_t ws_size, hipStream_t stream) {
  const u16* x = (const u16*)d_in[0];
  const u16* wqkv = (const u16*)d_in[1];
  const u16* wo = (const u16*)d_in[2];
  const u16* w1 = (const u16*)d_in[4];
  const u16* b1 = (const u16*)d_in[5];
  const u16* w2 = (const u16*)d_in[6];
  const u16* b2 = (const u16*)d_in[7];
  const u16* gate_w = (const u16*)d_in[8];
  char* ws = (char*)d_ws;
  u16* obuf = (u16*)(ws);
  u16* hbuf = (u16*)(ws + (2 << 20));
  u16* ubuf = (u16*)(ws + (6 << 20));
  float* gbuf = (float*)(ws + (8 << 20));
  unsigned* flags = (unsigned*)(ws + (9 << 20));
  k1_qkv_attn_ln<<<256, 512, 0, stream>>>(x, wqkv, gate_w, ws, w1, wo);
  k2_mlp<<<256, 512, 0, stream>>>(hbuf, w1, b1, ubuf, obuf, wo, w2, b2, gbuf, (u16*)d_out, flags);
}
```
